```python
import torch

def _softmax(x, axis=-1):
    return torch.softmax(x, dim=axis)
def _gelu(x):
    return torch.nn.functional.gelu(x, approximate="tanh")
def _silu(x):
    return torch.nn.functional.silu(x)
def _sigmoid(x):
    return torch.sigmoid(x)
def _layernorm(x, axis=-1, eps=1e-5):
    m = x.mean(dim=axis, keepdim=True)
    v = x.var(dim=axis, keepdim=True, unbiased=False)
    return (x - m) / torch.sqrt(v + eps)
def _scatter_add(tgt, idx, src, axis=0):
    return tgt.clone().index_add_(axis, idx, src)
def _segment_sum(src, seg, n):
    out = torch.zeros((n,) + src.shape[1:], dtype=src.dtype)
    return out.index_add_(0, seg, src)
def _pad(x, pw):
    flat = tuple(v for pair in reversed(pw) for v in pair)
    return torch.nn.functional.pad(x, flat)

kernel_name = 'standalone__kv_cache_quant_int4_int2_kv_with_per_channel_or_per_token_scales__sampling'

def setup_inputs():

    _w = (lambda a: (lambda t: t.to(torch.bfloat16) if t.is_floating_point() else t)(torch.as_tensor(a)))
    B = 8
    Sq = 32
    Skv = 8192
    Hq = 32
    Hkv = 8
    Dh = 128
    bits = 4
    G = 64
    if G <= 0 or G >= Skv:
        G = max(1, Skv // 2)
    Skv = (Skv // G) * G
    ngk = Skv // G
    qmax = (1 << bits) - 1
    return {
        "q": _w((lambda *s: torch.randn(*s, dtype=torch.bfloat16) * 1.6)(B, Hq, Sq, Dh)),
        "qk": _w(torch.randint(0, qmax + 1, (B, Hkv, Skv, Dh)).to(torch.int32)),
        "qv": _w(torch.randint(0, qmax + 1, (B, Hkv, Skv, Dh)).to(torch.int32)),
        "k_scales": _w(torch.abs((lambda *s: torch.randn(*s, dtype=torch.bfloat16) * 1.6)(B, Hkv, ngk, Dh)) * 0.05 + 0.02),
        "k_zeros": _w((lambda *s: torch.randn(*s, dtype=torch.bfloat16) * 1.6)(B, Hkv, ngk, Dh) + float(qmax) / 2.0),
        "v_scales": _w(torch.abs((lambda *s: torch.randn(*s, dtype=torch.bfloat16) * 0.05)(B, Hkv, Skv, 1)) * 0.05 + 0.02),
        "v_zeros": _w((lambda *s: torch.randn(*s, dtype=torch.bfloat16) * 0.05)(B, Hkv, Skv, 1) + float(qmax) / 2.0),
    }

def reference(q, qk, qv, k_scales, k_zeros, v_scales, v_zeros):
    B, Hq, Sq, Dh = q.shape
    Hkv, Skv = qk.shape[1], qk.shape[2]
    ngk = k_scales.shape[2]
    G = Skv // ngk
    ks = (lambda x, n, axis: torch.repeat_interleave(x, n, dim=axis))(k_scales, G, axis=2)
    kz = (lambda x, n, axis: torch.repeat_interleave(x, n, dim=axis))(k_zeros, G, axis=2)
    k = ((lambda x, dt: x.to(dt))(qk, q.dtype) - kz) * ks
    v = ((lambda x, dt: x.to(dt))(qv, q.dtype) - v_zeros) * v_scales
    rep = Hq // Hkv
    if rep > 1:
        k = (lambda x, n, axis: torch.repeat_interleave(x, n, dim=axis))(k, rep, axis=1)
        v = (lambda x, n, axis: torch.repeat_interleave(x, n, dim=axis))(v, rep, axis=1)
    scale = float(Dh) ** -0.5
    logits = torch.einsum("bhqd,bhkd->bhqk", q, k) * scale
    p = _softmax(logits, axis=-1)
    o = torch.einsum("bhqk,bhkd->bhqd", p, v)
    return o
```

```cpp
#include <hip/hip_runtime.h>
#include <hip/hip_bf16.h>
#include <stdint.h>

typedef __attribute__((ext_vector_type(8))) __bf16 bf16x8;
typedef __attribute__((ext_vector_type(2))) __bf16 bf16x2;
typedef __attribute__((ext_vector_type(16))) float f32x16;
typedef __attribute__((ext_vector_type(4))) float f32x4;
typedef __attribute__((ext_vector_type(2))) float f32x2;
typedef __attribute__((ext_vector_type(4))) unsigned u32x4;
typedef __attribute__((ext_vector_type(2))) unsigned u32x2;

constexpr int NB = 8, SQ = 32, SKV = 8192, HQ = 32, HKV = 8, DH = 128, GRP = 64, NGK = SKV / GRP;
constexpr int REP = HQ / HKV;
constexpr int NSPLIT = 4;
constexpr int TOKW = SKV / NSPLIT;
constexpr int TILE = 64;
constexpr int NT = TOKW / TILE;
constexpr int NPAIR = NB * HKV;
constexpr int TILE_BYTES = TILE * DH * 4;
constexpr int CHUNK_BYTES = TOKW * DH * 4;

constexpr int KROW = 272;
constexpr int VROW = 144;
constexpr int KBUF = TILE * KROW;
constexpr int VBUF = DH * VROW;
constexpr int L_K = 0;
constexpr int L_V = L_K + 2 * KBUF;
constexpr int L_KC1 = L_V + 2 * VBUF;
constexpr int L_KC0 = L_KC1 + NT * DH * 4;
constexpr int L_VC1 = L_KC0 + NT * DH * 4;
constexpr int L_VC0 = L_VC1 + TOKW * 4;
constexpr int L_ML = L_VC0 + TOKW * 4;
constexpr int L_END = L_ML + 8 * 32 * 8;
static_assert(L_END <= 160 * 1024, "LDS");
static_assert(8 * 8192 <= L_KC1, "epilogue exchange region reuses the tile buffers");

constexpr float SL2E = 0.08838834764831845f * 1.4426950408889634f;
constexpr float THR = 8.0f;
constexpr float TWO24 = 16777216.0f;

constexpr size_t WS_PO = 0;
constexpr size_t WS_PML = WS_PO + (size_t)NPAIR * NSPLIT * 128 * DH * 4;

__device__ __forceinline__ float bf_lo(unsigned x) { return __builtin_bit_cast(float, x << 16); }
__device__ __forceinline__ float bf_hi(unsigned x) { return __builtin_bit_cast(float, x & 0xffff0000u); }
__device__ __forceinline__ unsigned pk_bf16(float lo, float hi) {
  f32x2 v = {lo, hi};
  return __builtin_bit_cast(unsigned, __builtin_convertvector(v, bf16x2));
}
__device__ __forceinline__ float deq(unsigned code, float c1, float c0) {
  float r;
  asm("v_fma_mix_f32 %0, %1, %2, %3 op_sel_hi:[1,0,0]" : "=v"(r) : "v"(code), "v"(c1), "v"(c0));
  return r;
}

__global__ __launch_bounds__(512, 2) void kivi_attn_split(const __hip_bfloat16* __restrict__ q, const int* __restrict__ qk,
                                                          const int* __restrict__ qv, const __hip_bfloat16* __restrict__ ksc,
                                                          const __hip_bfloat16* __restrict__ kzr, const __hip_bfloat16* __restrict__ vsc,
                                                          const __hip_bfloat16* __restrict__ vzr, float* __restrict__ po,
                                                          float* __restrict__ pml) {
  extern __shared__ __attribute__((aligned(16))) char smem[];
  const int tid = threadIdx.x, lane = tid & 63, r = lane & 31, h = lane >> 5;
  const int w = __builtin_amdgcn_readfirstlane(tid >> 6);
  const int qb = w & 3, th = w >> 2;
  const int bid = blockIdx.x;
  const int pair = bid >> 2, split = bid & 3;

  const size_t kvoff = ((size_t)pair * SKV + (size_t)split * TOKW) * DH;
  const auto rk = __builtin_amdgcn_make_buffer_rsrc((void*)(qk + kvoff), 0, CHUNK_BYTES, 0x00020000);
  const auto rv = __builtin_amdgcn_make_buffer_rsrc((void*)(qv + kvoff), 0, CHUNK_BYTES, 0x00020000);

  const size_t goff = ((size_t)pair * NGK + (size_t)split * NT) * DH;
  const u32x4 ks_raw = *reinterpret_cast<const u32x4*>(ksc + goff + tid * 8);
  const u32x4 kz_raw = *reinterpret_cast<const u32x4*>(kzr + goff + tid * 8);
  const size_t toff = (size_t)pair * SKV + (size_t)split * TOKW;
  const u32x2 vs_raw = *reinterpret_cast<const u32x2*>(vsc + toff + tid * 4);
  const u32x2 vz_raw = *reinterpret_cast<const u32x2*>(vzr + toff + tid * 4);
  bf16x8 qf[8];
  {
    const __hip_bfloat16* qrow = q + ((size_t)(pair * REP + qb) * SQ + r) * DH + h * 8;
#pragma unroll
    for (int s = 0; s < 8; ++s) qf[s] = *reinterpret_cast<const bf16x8*>(qrow + s * 16);
  }

  int kvo = w * 4096 + lane * 16;
  int vvo = w * 4096 + h * 2048 + r * 16;
  u32x4 rk0[4], rv0[4], rk1[4], rv1[4];
#define ISSUE(RK, RV)                                                                                         \
  do {                                                                                                        \
    _Pragma("unroll") for (int i = 0; i < 4; ++i) RK[i] = __builtin_amdgcn_raw_buffer_load_b128(rk, kvo + i * 1024, 0, 2); \
    _Pragma("unroll") for (int i = 0; i < 4; ++i) RV[i] = __builtin_amdgcn_raw_buffer_load_b128(rv, vvo + i * 512, 0, 2);  \
    kvo += TILE_BYTES; vvo += TILE_BYTES;                                                                     \
  } while (0)
  ISSUE(rk0, rv0);
  ISSUE(rk1, rv1);

  {
    f32x4 a0, a1, b0, b1;
#pragma unroll
    for (int j = 0; j < 2; ++j) {
      const float s0 = bf_lo(ks_raw[j]), s1 = bf_hi(ks_raw[j]), s2 = bf_lo(ks_raw[2 + j]), s3 = bf_hi(ks_raw[2 + j]);
      a0[2 * j] = s0 * TWO24; a0[2 * j + 1] = s1 * TWO24; a1[2 * j] = s2 * TWO24; a1[2 * j + 1] = s3 * TWO24;
      b0[2 * j] = -bf_lo(kz_raw[j]) * s0; b0[2 * j + 1] = -bf_hi(kz_raw[j]) * s1;
      b1[2 * j] = -bf_lo(kz_raw[2 + j]) * s2; b1[2 * j + 1] = -bf_hi(kz_raw[2 + j]) * s3;
    }
    *reinterpret_cast<f32x4*>(smem + L_KC1 + tid * 32) = a0;
    *reinterpret_cast<f32x4*>(smem + L_KC1 + tid * 32 + 16) = a1;
    *reinterpret_cast<f32x4*>(smem + L_KC0 + tid * 32) = b0;
    *reinterpret_cast<f32x4*>(smem + L_KC0 + tid * 32 + 16) = b1;
    f32x4 c, d;
#pragma unroll
    for (int j = 0; j < 2; ++j) {
      const float s0 = bf_lo(vs_raw[j]), s1 = bf_hi(vs_raw[j]);
      c[2 * j] = s0 * TWO24; c[2 * j + 1] = s1 * TWO24;
      d[2 * j] = -bf_lo(vz_raw[j]) * s0; d[2 * j + 1] = -bf_hi(vz_raw[j]) * s1;
    }
    *reinterpret_cast<f32x4*>(smem + L_VC1 + tid * 16) = c;
    *reinterpret_cast<f32x4*>(smem + L_VC0 + tid * 16) = d;
  }
#define BARRIER()                                          \
  do {                                                     \
    asm volatile("s_waitcnt lgkmcnt(0)" ::: "memory");     \
    __builtin_amdgcn_s_barrier();                          \
    asm volatile("" ::: "memory");                         \
  } while (0)
  BARRIER();

  const int kw_off = L_K + (8 * w + h) * KROW + r * 8;
  const int vw_off = L_V + r * VROW + 32 * (w >> 1) + 16 * h + 8 * (w & 1);
  const int kr_off = L_K + (32 * th + r) * KROW + h * 16;
  const int vr_off = L_V + r * VROW + 64 * th + 16 * h;
  const int kc_off = r * 16;
  const int vc_off = (8 * w + 4 * h) * 4;

#define DEQUANT(RK, RV, BUF, T)                                                                       \
  do {                                                                                                \
    const f32x4 kc1 = *reinterpret_cast<const f32x4*>(smem + L_KC1 + kc_off + (T) * 512);             \
    const f32x4 kc0 = *reinterpret_cast<const f32x4*>(smem + L_KC0 + kc_off + (T) * 512);             \
    const f32x4 vc1 = *reinterpret_cast<const f32x4*>(smem + L_VC1 + vc_off + (T) * 256);             \
    const f32x4 vc0 = *reinterpret_cast<const f32x4*>(smem + L_VC0 + vc_off + (T) * 256);             \
    _Pragma("unroll") for (int i = 0; i < 4; ++i) {                                                   \
      u32x2 o;                                                                                        \
      o[0] = pk_bf16(deq(RK[i][0], kc1[0], kc0[0]), deq(RK[i][1], kc1[1], kc0[1]));                   \
      o[1] = pk_bf16(deq(RK[i][2], kc1[2], kc0[2]), deq(RK[i][3], kc1[3], kc0[3]));                   \
      *reinterpret_cast<u32x2*>(smem + kw_off + 2 * i * KROW + (BUF) * KBUF) = o;                     \
    }                                                                                                 \
    _Pragma("unroll") for (int c = 0; c < 4; ++c) {                                                   \
      u32x2 o;                                                                                        \
      o[0] = pk_bf16(deq(RV[0][c], vc1[0], vc0[0]), deq(RV[1][c], vc1[1], vc0[1]));                   \
      o[1] = pk_bf16(deq(RV[2][c], vc1[2], vc0[2]), deq(RV[3][c], vc1[3], vc0[3]));                   \
      *reinterpret_cast<u32x2*>(smem + vw_off + c * 32 * VROW + (BUF) * VBUF) = o;                    \
    }                                                                                                 \
  } while (0)

  f32x16 o[4];
#pragma unroll
  for (int c = 0; c < 4; ++c) o[c] = f32x16{};
  float m = -__builtin_inff(), l = 0.f;

#define COMPUTE(BUF)                                                                                  \
  do {                                                                                                \
    f32x16 s = f32x16{};                                                                              \
    _Pragma("unroll") for (int k = 0; k < 8; ++k) {                                                   \
      const bf16x8 kf = *reinterpret_cast<const bf16x8*>(smem + kr_off + k * 32 + (BUF) * KBUF);      \
      s = __builtin_amdgcn_mfma_f32_32x32x16_bf16(kf, qf[k], s, 0, 0, 0);                             \
    }                                                                                                 \
    float tm = fmaxf(fmaxf(s[0], s[1]), s[2]);                                                        \
    _Pragma("unroll") for (int k = 3; k < 15; k += 2) tm = fmaxf(fmaxf(tm, s[k]), s[k + 1]);          \
    tm = fmaxf(tm, s[15]) * SL2E;                                                                     \
    if (__builtin_expect(__any(tm > m + THR), 0)) {                                                   \
      const auto sw = __builtin_amdgcn_permlane32_swap(__float_as_uint(tm), __float_as_uint(tm), false, false); \
      const float mn = fmaxf(m, fmaxf(__uint_as_float(sw[0]), __uint_as_float(sw[1])));               \
      const float al = __builtin_amdgcn_exp2f(m - mn);                                                \
      m = mn; l *= al;                                                                                \
      _Pragma("unroll") for (int c = 0; c < 4; ++c) o[c] *= al;                                       \
    }                                                                                                 \
    float ls = 0.f;                                                                                   \
    _Pragma("unroll") for (int k = 0; k < 16; ++k) { s[k] = __builtin_amdgcn_exp2f(fmaf(s[k], SL2E, -m)); ls += s[k]; } \
    l += ls;                                                                                          \
    _Pragma("unroll") for (int s2 = 0; s2 < 2; ++s2) {                                                \
      u32x4 pw;                                                                                       \
      _Pragma("unroll") for (int k = 0; k < 4; ++k) pw[k] = pk_bf16(s[8 * s2 + 2 * k], s[8 * s2 + 2 * k + 1]); \
      const bf16x8 pf = __builtin_bit_cast(bf16x8, pw);                                               \
      _Pragma("unroll") for (int c = 0; c < 4; ++c) {                                                 \
        const bf16x8 vf = *reinterpret_cast<const bf16x8*>(smem + vr_off + c * 32 * VROW + s2 * 32 + (BUF) * VBUF); \
        o[c] = __builtin_amdgcn_mfma_f32_32x32x16_bf16(vf, pf, o[c], 0, 0, 0);                        \
      }                                                                                               \
    }                                                                                                 \
  } while (0)

  DEQUANT(rk0, rv0, 0, 0);
  ISSUE(rk0, rv0);
  BARRIER();
  for (int t = 0; t < NT - 2; t += 2) {
    COMPUTE(0);
    DEQUANT(rk1, rv1, 1, t + 1);
    ISSUE(rk1, rv1);
    BARRIER();
    COMPUTE(1);
    DEQUANT(rk0, rv0, 0, t + 2);
    ISSUE(rk0, rv0);
    BARRIER();
  }
  COMPUTE(0);
  DEQUANT(rk1, rv1, 1, NT - 1);
  BARRIER();
  COMPUTE(1);

  {
    const auto sw = __builtin_amdgcn_permlane32_swap(__float_as_uint(l), __float_as_uint(l), false, false);
    l = __uint_as_float(sw[0]) + __uint_as_float(sw[1]);
  }
  if (h == 0) *reinterpret_cast<f32x2*>(smem + L_ML + (w * 32 + r) * 8) = f32x2{m, l};
  BARRIER();
  const f32x2 ot = *reinterpret_cast<const f32x2*>(smem + L_ML + ((w ^ 4) * 32 + r) * 8);
  const float mm = fmaxf(m, ot[0]);
  const float fa = __builtin_amdgcn_exp2f(m - mm), fb = __builtin_amdgcn_exp2f(ot[0] - mm);
  const float lt = l * fa + ot[1] * fb;
#pragma unroll
  for (int g = 0; g < 8; ++g) {
    const int ra = 8 + g, rb = g;
    f32x4 x;
#pragma unroll
    for (int c = 0; c < 4; ++c) x[c] = (th == 0 ? o[c][ra] : o[c][rb]) * fa;
    *reinterpret_cast<f32x4*>(smem + w * 8192 + g * 1024 + lane * 16) = x;
  }
  BARRIER();
  float* pob = po + ((size_t)(pair * NSPLIT + split) * 4 + qb) * (32 * DH);
#pragma unroll
  for (int g = 0; g < 8; ++g) {
    const f32x4 y = *reinterpret_cast<const f32x4*>(smem + (w ^ 4) * 8192 + g * 1024 + lane * 16);
    f32x4 x;
#pragma unroll
    for (int c = 0; c < 4; ++c) x[c] = (th == 0 ? o[c][g] : o[c][8 + g]) * fa + y[c];
    *reinterpret_cast<f32x4*>(pob + ((8 * th + g) * 64 + lane) * 4) = x;
  }
  if (th == 0 && h == 0)
    *reinterpret_cast<f32x2*>(pml + ((size_t)(pair * NSPLIT + split) * 128 + qb * 32 + r) * 2) = f32x2{mm, lt};
}

__global__ __launch_bounds__(256) void kivi_attn_combine(const float* __restrict__ po, const float* __restrict__ pml,
                                                         __hip_bfloat16* __restrict__ out) {
  const int pair = blockIdx.x >> 2, qb = blockIdx.x & 3;
  const int lane = threadIdx.x & 63, a = threadIdx.x >> 6, r = lane & 31, h = lane >> 5;
  f32x4 x[NSPLIT][4];
  f32x2 ml[NSPLIT];
#pragma unroll
  for (int s = 0; s < NSPLIT; ++s) {
    ml[s] = *reinterpret_cast<const f32x2*>(pml + ((size_t)(pair * NSPLIT + s) * 128 + qb * 32 + r) * 2);
    const float* pob = po + ((size_t)(pair * NSPLIT + s) * 4 + qb) * (32 * DH);
#pragma unroll
    for (int g = 0; g < 4; ++g) x[s][g] = *reinterpret_cast<const f32x4*>(pob + ((4 * a + g) * 64 + lane) * 4);
  }
  float mm = ml[0][0];
#pragma unroll
  for (int s = 1; s < NSPLIT; ++s) mm = fmaxf(mm, ml[s][0]);
  float f[NSPLIT], L = 0.f;
#pragma unroll
  for (int s = 0; s < NSPLIT; ++s) { f[s] = __builtin_amdgcn_exp2f(ml[s][0] - mm); L += f[s] * ml[s][1]; }
  const float inv = 1.0f / L;
  u32x4 w0, w1;
#pragma unroll
  for (int g = 0; g < 4; ++g) {
    f32x4 y = x[0][g] * f[0];
#pragma unroll
    for (int s = 1; s < NSPLIT; ++s) y += x[s][g] * f[s];
    y *= inv;
    const unsigned p0 = pk_bf16(y[0], y[1]), p1 = pk_bf16(y[2], y[3]);
    if (g < 2) { w0[2 * g] = p0; w0[2 * g + 1] = p1; } else { w1[2 * (g - 2)] = p0; w1[2 * (g - 2) + 1] = p1; }
  }
  __hip_bfloat16* orow = out + ((size_t)(pair * REP + qb) * SQ + r) * DH + 32 * a + 16 * h;
  *reinterpret_cast<u32x4*>(orow) = w0;
  *reinterpret_cast<u32x4*>(orow + 8) = w1;
}

extern "C" void kernel_launch(void* const* d_in, const int* in_sizes, int n_in, void* d_out, int out_size, void* d_ws,
                              size_t ws_size, hipStream_t stream) {
  const __hip_bfloat16* q = (const __hip_bfloat16*)d_in[0];
  const int* qk = (const int*)d_in[1];
  const int* qv = (const int*)d_in[2];
  const __hip_bfloat16* ksc = (const __hip_bfloat16*)d_in[3];
  const __hip_bfloat16* kzr = (const __hip_bfloat16*)d_in[4];
  const __hip_bfloat16* vsc = (const __hip_bfloat16*)d_in[5];
  const __hip_bfloat16* vzr = (const __hip_bfloat16*)d_in[6];
  float* po = (float*)((char*)d_ws + WS_PO);
  float* pml = (float*)((char*)d_ws + WS_PML);
  (void)hipFuncSetAttribute((const void*)kivi_attn_split, hipFuncAttributeMaxDynamicSharedMemorySize, L_END);
  hipLaunchKernelGGL(kivi_attn_split, dim3(NPAIR * NSPLIT), dim3(512), L_END, stream, q, qk, qv, ksc, kzr, vsc, vzr, po, pml);
  hipLaunchKernelGGL(kivi_attn_combine, dim3(NPAIR * 4), dim3(256), 0, stream, (const float*)po, (const float*)pml,
                     (__hip_bfloat16*)d_out);
}
```

```cpp
#include <hip/hip_runtime.h>
#include <hip/hip_bf16.h>
#include <stdint.h>

typedef __attribute__((ext_vector_type(8))) __bf16 bf16x8;
typedef __attribute__((ext_vector_type(2))) __bf16 bf16x2;
typedef __attribute__((ext_vector_type(4))) float f32x4;
typedef __attribute__((ext_vector_type(2))) float f32x2;
typedef __attribute__((ext_vector_type(4))) unsigned u32x4;
typedef __attribute__((ext_vector_type(2))) unsigned u32x2;
typedef unsigned long long u64;

constexpr int NB = 8, SQ = 32, SKV = 8192, HQ = 32, HKV = 8, DH = 128, GRP = 64, NGK = SKV / GRP;
constexpr int REP = HQ / HKV;
constexpr int NSPLIT = 4;
constexpr int TOKW = SKV / NSPLIT;
constexpr int TILE = 64;
constexpr int NT = TOKW / TILE;
constexpr int NPAIR = NB * HKV;
constexpr int TILE_BYTES = TILE * DH * 4;
constexpr int CHUNK_BYTES = TOKW * DH * 4;

constexpr int KROW = 288;
constexpr int KBUF = TILE * KROW;
constexpr int VBUF = DH * 128;
constexpr int L_K = 0;
constexpr int L_V = L_K + 2 * KBUF;
constexpr int L_KC1 = L_V + 2 * VBUF;
constexpr int L_KC0 = L_KC1 + NT * DH * 4;
constexpr int L_VC1 = L_KC0 + NT * DH * 4;
constexpr int L_VC0 = L_VC1 + TOKW * 4;
constexpr int L_Q = L_VC0 + TOKW * 4;
constexpr int L_MISC = L_Q + 128 * KROW;
constexpr int L_END = L_MISC + 256;
static_assert(L_END <= 160 * 1024, "LDS");

constexpr float SL2E = 0.08838834764831845f * 1.4426950408889634f;
constexpr float THR = 8.0f;
constexpr float TWO24 = 16777216.0f;

constexpr size_t WS_SYNC = 0;
constexpr size_t WS_LSE = WS_SYNC + (size_t)NPAIR * 1024;
constexpr size_t WS_PO = WS_LSE + (size_t)NPAIR * 16 * 128;
constexpr size_t WS_END = WS_PO + (size_t)NPAIR * 16 * 16384;

__device__ __forceinline__ float bf_lo(unsigned x) { return __builtin_bit_cast(float, x << 16); }
__device__ __forceinline__ float bf_hi(unsigned x) { return __builtin_bit_cast(float, x & 0xffff0000u); }
__device__ __forceinline__ unsigned pk_bf16(float lo, float hi) {
  f32x2 v = {lo, hi};
  return __builtin_bit_cast(unsigned, __builtin_convertvector(v, bf16x2));
}
__device__ __forceinline__ float deq(unsigned code, float c1, float c0) {
  float r;
  asm("v_fma_mix_f32 %0, %1, %2, %3 op_sel_hi:[1,0,0]" : "=v"(r) : "v"(code), "v"(c1), "v"(c0));
  return r;
}
__device__ __forceinline__ float xg_max(float x) {
  auto s = __builtin_amdgcn_permlane16_swap(__float_as_uint(x), __float_as_uint(x), false, false);
  x = fmaxf(__uint_as_float(s[0]), __uint_as_float(s[1]));
  auto t = __builtin_amdgcn_permlane32_swap(__float_as_uint(x), __float_as_uint(x), false, false);
  return fmaxf(__uint_as_float(t[0]), __uint_as_float(t[1]));
}
__device__ __forceinline__ float xg_sum(float x) {
  auto s = __builtin_amdgcn_permlane16_swap(__float_as_uint(x), __float_as_uint(x), false, false);
  x = __uint_as_float(s[0]) + __uint_as_float(s[1]);
  auto t = __builtin_amdgcn_permlane32_swap(__float_as_uint(x), __float_as_uint(x), false, false);
  return __uint_as_float(t[0]) + __uint_as_float(t[1]);
}
#define NTLD4(p) __builtin_nontemporal_load(reinterpret_cast<const u32x4*>(p))
#define NTLD2(p) __builtin_nontemporal_load(reinterpret_cast<const u32x2*>(p))
#define RLX_AGENT __ATOMIC_RELAXED, __HIP_MEMORY_SCOPE_AGENT

__global__ __launch_bounds__(512, 2) void kivi_attn(const __hip_bfloat16* __restrict__ q, const int* __restrict__ qk,
                                                    const int* __restrict__ qv, const __hip_bfloat16* __restrict__ ksc,
                                                    const __hip_bfloat16* __restrict__ kzr, const __hip_bfloat16* __restrict__ vsc,
                                                    const __hip_bfloat16* __restrict__ vzr, char* __restrict__ ws,
                                                    __hip_bfloat16* __restrict__ out) {
  extern __shared__ __attribute__((aligned(16))) char smem[];
  const int tid = threadIdx.x, lane = tid & 63, r = lane & 31, h = lane >> 5, c16 = lane & 15, g = lane >> 4;
  const int w = __builtin_amdgcn_readfirstlane(tid >> 6);
  const int bid = blockIdx.x;
  const int pair = bid >> 2, split = bid & 3;

  const size_t kvoff = ((size_t)pair * SKV + (size_t)split * TOKW) * DH;
  const auto rk = __builtin_amdgcn_make_buffer_rsrc((void*)(qk + kvoff), 0, CHUNK_BYTES, 0x00020000);
  const auto rv = __builtin_amdgcn_make_buffer_rsrc((void*)(qv + kvoff), 0, CHUNK_BYTES, 0x00020000);
  u64* sync = reinterpret_cast<u64*>(ws + WS_SYNC + (size_t)pair * 1024);

  int vzero = 0;
  asm volatile("" : "+v"(vzero));
  const u64 esrc = __hip_atomic_load(sync + vzero, RLX_AGENT);
  const size_t goff = ((size_t)pair * NGK + (size_t)split * NT) * DH;
  const u32x4 ks_raw = NTLD4(ksc + goff + tid * 8);
  const u32x4 kz_raw = NTLD4(kzr + goff + tid * 8);
  const size_t toff = (size_t)pair * SKV + (size_t)split * TOKW;
  const u32x2 vs_raw = NTLD2(vsc + toff + tid * 4);
  const u32x2 vz_raw = NTLD2(vzr + toff + tid * 4);
  u32x4 q_raw[4];
  {
    const __hip_bfloat16* qp = q + (size_t)pair * (REP * SQ * DH) + w * 2048 + lane * 8;
#pragma unroll
    for (int i = 0; i < 4; ++i) q_raw[i] = NTLD4(qp + i * 512);
  }

  int kvo = w * 4096 + lane * 16;
  int vvo = w * 4096 + h * 2048 + r * 16;
  u32x4 rk0[4], rv0[4], rk1[4], rv1[4];
#define ISSUE(RK, RV)                                                                                         \
  do {                                                                                                        \
    _Pragma("unroll") for (int i = 0; i < 4; ++i) RK[i] = __builtin_amdgcn_raw_buffer_load_b128(rk, kvo + i * 1024, 0, 2); \
    _Pragma("unroll") for (int i = 0; i < 4; ++i) RV[i] = __builtin_amdgcn_raw_buffer_load_b128(rv, vvo + i * 512, 0, 2);  \
    kvo += TILE_BYTES; vvo += TILE_BYTES;                                                                     \
  } while (0)
  __builtin_amdgcn_sched_barrier(0);
  ISSUE(rk0, rv0);
  __builtin_amdgcn_sched_barrier(0);
  ISSUE(rk1, rv1);
  __builtin_amdgcn_sched_barrier(0);

  if (tid == 0) *reinterpret_cast<u64*>(smem + L_MISC) = esrc * 0x9E3779B97F4A7C15ull + 0xD1B54A32D192ED03ull;
  {
    f32x4 a0, a1, b0, b1;
#pragma unroll
    for (int j = 0; j < 2; ++j) {
      const float s0 = bf_lo(ks_raw[j]), s1 = bf_hi(ks_raw[j]), s2 = bf_lo(ks_raw[2 + j]), s3 = bf_hi(ks_raw[2 + j]);
      a0[2 * j] = s0 * TWO24; a0[2 * j + 1] = s1 * TWO24; a1[2 * j] = s2 * TWO24; a1[2 * j + 1] = s3 * TWO24;
      b0[2 * j] = -bf_lo(kz_raw[j]) * s0; b0[2 * j + 1] = -bf_hi(kz_raw[j]) * s1;
      b1[2 * j] = -bf_lo(kz_raw[2 + j]) * s2; b1[2 * j + 1] = -bf_hi(kz_raw[2 + j]) * s3;
    }
    *reinterpret_cast<f32x4*>(smem + L_KC1 + tid * 32) = a0;
    *reinterpret_cast<f32x4*>(smem + L_KC1 + tid * 32 + 16) = a1;
    *reinterpret_cast<f32x4*>(smem + L_KC0 + tid * 32) = b0;
    *reinterpret_cast<f32x4*>(smem + L_KC0 + tid * 32 + 16) = b1;
    f32x4 c, d;
#pragma unroll
    for (int j = 0; j < 2; ++j) {
      const float s0 = bf_lo(vs_raw[j]), s1 = bf_hi(vs_raw[j]);
      c[2 * j] = s0 * TWO24; c[2 * j + 1] = s1 * TWO24;
      d[2 * j] = -bf_lo(vz_raw[j]) * s0; d[2 * j + 1] = -bf_hi(vz_raw[j]) * s1;
    }
    *reinterpret_cast<f32x4*>(smem + L_VC1 + tid * 16) = c;
    *reinterpret_cast<f32x4*>(smem + L_VC0 + tid * 16) = d;
#pragma unroll
    for (int i = 0; i < 4; ++i)
      *reinterpret_cast<u32x4*>(smem + L_Q + (16 * w + 4 * i + g) * KROW + c16 * 16) = q_raw[i];
  }
#define BARRIER()                                          \
  do {                                                     \
    asm volatile("s_waitcnt lgkmcnt(0)" ::: "memory");     \
    __builtin_amdgcn_s_barrier();                          \
    asm volatile("" ::: "memory");                         \
  } while (0)
  BARRIER();
  bf16x8 qf[4];
#pragma unroll
  for (int k = 0; k < 4; ++k) qf[k] = *reinterpret_cast<const bf16x8*>(smem + L_Q + (16 * w + c16) * KROW + (4 * k + g) * 16);

  const int kw_off = L_K + (8 * w + h) * KROW + r * 8;
  const int vw_off = L_V + r * 128 + (((4 * (w >> 2) + 2 * (w & 1) + h) ^ ((r >> 1) & 7)) << 4) + 8 * ((w >> 1) & 1);
  const int kr_off = L_K + c16 * KROW + g * 16;
  const int vr_off0 = L_V + c16 * 128 + ((g ^ ((c16 >> 1) & 7)) << 4);
  const int vr_off1 = L_V + c16 * 128 + (((4 + g) ^ ((c16 >> 1) & 7)) << 4);
  const int kc_off = r * 16;
  const int vc_off = (8 * w + 4 * h) * 4;

#define DEQUANT(RK, RV, BUF, T)                                                                       \
  do {                                                                                                \
    const f32x4 kc1 = *reinterpret_cast<const f32x4*>(smem + L_KC1 + kc_off + (T) * 512);             \
    const f32x4 kc0 = *reinterpret_cast<const f32x4*>(smem + L_KC0 + kc_off + (T) * 512);             \
    const f32x4 vc1 = *reinterpret_cast<const f32x4*>(smem + L_VC1 + vc_off + (T) * 256);             \
    const f32x4 vc0 = *reinterpret_cast<const f32x4*>(smem + L_VC0 + vc_off + (T) * 256);             \
    _Pragma("unroll") for (int i = 0; i < 4; ++i) {                                                   \
      u32x2 o_;                                                                                       \
      o_[0] = pk_bf16(deq(RK[i][0], kc1[0], kc0[0]), deq(RK[i][1], kc1[1], kc0[1]));                  \
      o_[1] = pk_bf16(deq(RK[i][2], kc1[2], kc0[2]), deq(RK[i][3], kc1[3], kc0[3]));                  \
      *reinterpret_cast<u32x2*>(smem + kw_off + 2 * i * KROW + (BUF) * KBUF) = o_;                    \
    }                                                                                                 \
    _Pragma("unroll") for (int c = 0; c < 4; ++c) {                                                   \
      u32x2 o_;                                                                                       \
      o_[0] = pk_bf16(deq(RV[0][c], vc1[0], vc0[0]), deq(RV[1][c], vc1[1], vc0[1]));                  \
      o_[1] = pk_bf16(deq(RV[2][c], vc1[2], vc0[2]), deq(RV[3][c], vc1[3], vc0[3]));                  \
      *reinterpret_cast<u32x2*>(smem + vw_off + c * 4096 + (BUF) * VBUF) = o_;                        \
    }                                                                                                 \
  } while (0)

  f32x4 o[8];
#pragma unroll
  for (int e = 0; e < 8; ++e) o[e] = f32x4{};
  float m = -__builtin_inff(), l = 0.f;

#define COMPUTE(BUF)                                                                                  \
  do {                                                                                                \
    f32x4 s[4];                                                                                       \
    _Pragma("unroll") for (int tb = 0; tb < 4; ++tb) {                                                \
      s[tb] = f32x4{};                                                                                \
      _Pragma("unroll") for (int k = 0; k < 4; ++k) {                                                 \
        const bf16x8 kf = *reinterpret_cast<const bf16x8*>(smem + kr_off + tb * 16 * KROW + k * 64 + (BUF) * KBUF); \
        s[tb] = __builtin_amdgcn_mfma_f32_16x16x32_bf16(kf, qf[k], s[tb], 0, 0, 0);                   \
      }                                                                                               \
    }                                                                                                 \
    float tm = fmaxf(fmaxf(fmaxf(s[0][0], s[0][1]), fmaxf(s[0][2], s[0][3])), fmaxf(fmaxf(s[1][0], s[1][1]), fmaxf(s[1][2], s[1][3]))); \
    tm = fmaxf(tm, fmaxf(fmaxf(fmaxf(s[2][0], s[2][1]), fmaxf(s[2][2], s[2][3])), fmaxf(fmaxf(s[3][0], s[3][1]), fmaxf(s[3][2], s[3][3])))); \
    tm *= SL2E;                                                                                       \
    if (__builtin_expect(__any(tm > m + THR), 0)) {                                                   \
      const float mn = fmaxf(m, xg_max(tm));                                                          \
      const float al = __builtin_amdgcn_exp2f(m - mn);                                                \
      m = mn; l *= al;                                                                                \
      _Pragma("unroll") for (int e = 0; e < 8; ++e) o[e] *= al;                                       \
    }                                                                                                 \
    float ls = 0.f;                                                                                   \
    _Pragma("unroll") for (int tb = 0; tb < 4; ++tb)                                                  \
      _Pragma("unroll") for (int i = 0; i < 4; ++i) { s[tb][i] = __builtin_amdgcn_exp2f(fmaf(s[tb][i], SL2E, -m)); ls += s[tb][i]; } \
    l += ls;                                                                                          \
    _Pragma("unroll") for (int u = 0; u < 2; ++u) {                                                   \
      u32x4 pw;                                                                                       \
      pw[0] = pk_bf16(s[2 * u][0], s[2 * u][1]); pw[1] = pk_bf16(s[2 * u][2], s[2 * u][3]);           \
      pw[2] = pk_bf16(s[2 * u + 1][0], s[2 * u + 1][1]); pw[3] = pk_bf16(s[2 * u + 1][2], s[2 * u + 1][3]); \
      const bf16x8 pf = __builtin_bit_cast(bf16x8, pw);                                               \
      _Pragma("unroll") for (int e = 0; e < 8; ++e) {                                                 \
        const bf16x8 vf = *reinterpret_cast<const bf16x8*>(smem + (u ? vr_off1 : vr_off0) + e * 2048 + (BUF) * VBUF); \
        o[e] = __builtin_amdgcn_mfma_f32_16x16x32_bf16(vf, pf, o[e], 0, 0, 0);                        \
      }                                                                                               \
    }                                                                                                 \
  } while (0)

  DEQUANT(rk0, rv0, 0, 0);
  ISSUE(rk0, rv0);
  BARRIER();
  for (int t = 0; t < NT - 4; t += 2) {
    COMPUTE(0);
    DEQUANT(rk1, rv1, 1, t + 1);
    ISSUE(rk1, rv1);
    BARRIER();
    COMPUTE(1);
    DEQUANT(rk0, rv0, 0, t + 2);
    ISSUE(rk0, rv0);
    BARRIER();
  }
  COMPUTE(0);
  DEQUANT(rk1, rv1, 1, NT - 3);
  ISSUE(rk1, rv1);
  BARRIER();
  COMPUTE(1);
  DEQUANT(rk0, rv0, 0, NT - 2);
  BARRIER();
  COMPUTE(0);
  DEQUANT(rk1, rv1, 1, NT - 1);
  BARRIER();
  COMPUTE(1);

  l = xg_sum(l);
  const float inv = 1.0f / l;
  const float lse = m + __builtin_amdgcn_logf(l);
  const u64 epoch = *reinterpret_cast<const u64*>(smem + L_MISC);
  const int dest = w >> 1;
  if (dest == split) {
#pragma unroll
    for (int k = 0; k < 8; ++k) {
      f32x4 x;
#pragma unroll
      for (int c = 0; c < 4; ++c) x[c] = o[2 * c + (k >> 2)][k & 3] * inv;
      *reinterpret_cast<f32x4*>(smem + L_Q + (w & 1) * 8192 + k * 1024 + lane * 16) = x;
    }
    if (g == 0) *reinterpret_cast<float*>(smem + L_MISC + 64 + ((w & 1) * 16 + c16) * 4) = lse;
  } else {
    const size_t slab = (size_t)(pair * 4 + dest) * 4 + split;
    const auto rpo = __builtin_amdgcn_make_buffer_rsrc((void*)(ws + WS_PO + slab * 16384 + (w & 1) * 8192), 0, 8192, 0x00020000);
#pragma unroll
    for (int k = 0; k < 8; ++k) {
      f32x4 x;
#pragma unroll
      for (int c = 0; c < 4; ++c) x[c] = o[2 * c + (k >> 2)][k & 3] * inv;
      __builtin_amdgcn_raw_buffer_store_b128(__builtin_bit_cast(u32x4, x), rpo, k * 1024 + lane * 16, 0, 16  );
    }
    if (g == 0) __hip_atomic_store(reinterpret_cast<float*>(ws + WS_LSE + slab * 128) + (w & 1) * 16 + c16, lse, RLX_AGENT);
    asm volatile("s_waitcnt vmcnt(0)" ::: "memory");
    if (lane == 0) __hip_atomic_store(sync + 16 * (1 + dest) + split * 2 + (w & 1), epoch, RLX_AGENT);
  }
  if (w == 2 * split) {
    const bool need = lane < 8 && (lane >> 1) != split;
    u64* fl = sync + 16 * (1 + split) + (lane & 7);
    for (int spin = 0; spin < (1 << 22); ++spin) {
      const u64 v = __hip_atomic_load(fl, RLX_AGENT);
      if (__all(!need || v == epoch)) break;
      __builtin_amdgcn_s_sleep(2);
    }
    if (split == 0 && lane == 0) __hip_atomic_store(sync, epoch, RLX_AGENT);
  }
  BARRIER();
  {
    const int hw = w >> 2, k0 = 2 * (w & 3);
    f32x4 x[3][2];
    float ls[3];
#pragma unroll
    for (int j = 0; j < 3; ++j) {
      const int src = j + (j >= split ? 1 : 0);
      const size_t slab = (size_t)(pair * 4 + split) * 4 + src;
      const auto rpo = __builtin_amdgcn_make_buffer_rsrc((void*)(ws + WS_PO + slab * 16384 + hw * 8192), 0, 8192, 0x00020000);
      x[j][0] = __builtin_bit_cast(f32x4, __builtin_amdgcn_raw_buffer_load_b128(rpo, k0 * 1024 + lane * 16, 0, 16));
      x[j][1] = __builtin_bit_cast(f32x4, __builtin_amdgcn_raw_buffer_load_b128(rpo, k0 * 1024 + 1024 + lane * 16, 0, 16));
      ls[j] = __hip_atomic_load(reinterpret_cast<const float*>(ws + WS_LSE + slab * 128) + hw * 16 + c16, RLX_AGENT);
    }
    f32x4 y0 = *reinterpret_cast<const f32x4*>(smem + L_Q + hw * 8192 + k0 * 1024 + lane * 16);
    f32x4 y1 = *reinterpret_cast<const f32x4*>(smem + L_Q + hw * 8192 + k0 * 1024 + 1024 + lane * 16);
    const float lo = *reinterpret_cast<const float*>(smem + L_MISC + 64 + (hw * 16 + c16) * 4);
    const float mx = fmaxf(fmaxf(lo, ls[0]), fmaxf(ls[1], ls[2]));
    const float w0 = __builtin_amdgcn_exp2f(lo - mx);
    float wsum = w0;
    y0 *= w0; y1 *= w0;
#pragma unroll
    for (int j = 0; j < 3; ++j) {
      const float wj = __builtin_amdgcn_exp2f(ls[j] - mx);
      wsum += wj; y0 += x[j][0] * wj; y1 += x[j][1] * wj;
    }
    const float iw = 1.0f / wsum;
    y0 *= iw; y1 *= iw;
    u32x4 ov = {pk_bf16(y0[0], y0[1]), pk_bf16(y0[2], y0[3]), pk_bf16(y1[0], y1[1]), pk_bf16(y1[2], y1[3])};
    __hip_bfloat16* op = out + ((size_t)(pair * REP + split) * SQ + hw * 16 + c16) * DH + 64 * (k0 >> 2) + 16 * g + 4 * (k0 & 3);
    *reinterpret_cast<u32x4*>(op) = ov;
  }
}

extern "C" void kernel_launch(void* const* d_in, const int* in_sizes, int n_in, void* d_out, int out_size, void* d_ws,
                              size_t ws_size, hipStream_t stream) {
  const __hip_bfloat16* q = (const __hip_bfloat16*)d_in[0];
  const int* qk = (const int*)d_in[1];
  const int* qv = (const int*)d_in[2];
  const __hip_bfloat16* ksc = (const __hip_bfloat16*)d_in[3];
  const __hip_bfloat16* kzr = (const __hip_bfloat16*)d_in[4];
  const __hip_bfloat16* vsc = (const __hip_bfloat16*)d_in[5];
  const __hip_bfloat16* vzr = (const __hip_bfloat16*)d_in[6];
  (void)hipFuncSetAttribute((const void*)kivi_attn, hipFuncAttributeMaxDynamicSharedMemorySize, L_END);
  hipLaunchKernelGGL(kivi_attn, dim3(NPAIR * NSPLIT), dim3(512), L_END, stream, q, qk, qv, ksc, kzr, vsc, vzr, (char*)d_ws,
                     (__hip_bfloat16*)d_out);
}
```

```cpp
#include <hip/hip_runtime.h>
#include <hip/hip_bf16.h>
#include <stdint.h>

typedef __attribute__((ext_vector_type(8))) __bf16 bf16x8;
typedef __attribute__((ext_vector_type(2))) __bf16 bf16x2;
typedef __attribute__((ext_vector_type(4))) float f32x4;
typedef __attribute__((ext_vector_type(2))) float f32x2;
typedef __attribute__((ext_vector_type(4))) unsigned u32x4;
typedef __attribute__((ext_vector_type(2))) unsigned u32x2;
typedef unsigned long long u64;

#define XMAP 0
#define QNT 1
#define OUT_AUX 0
#define POLL_SLEEP 2
constexpr int NB = 8, SQ = 32, SKV = 8192, HQ = 32, HKV = 8, DH = 128, GRP = 64, NGK = SKV / GRP;
constexpr int REP = HQ / HKV;
constexpr int NSPLIT = 4;
constexpr int TOKW = SKV / NSPLIT;
constexpr int TILE = 64;
constexpr int NT = TOKW / TILE;
constexpr int NPAIR = NB * HKV;
constexpr int TILE_BYTES = TILE * DH * 4;
constexpr int CHUNK_BYTES = TOKW * DH * 4;

constexpr int KROW = 288;
constexpr int KBUF = TILE * KROW;
constexpr int VBUF = DH * 128;
constexpr int L_K = 0;
constexpr int L_V = L_K + 2 * KBUF;
constexpr int L_KC1 = L_V + 2 * VBUF;
constexpr int L_KC0 = L_KC1 + NT * DH * 4;
constexpr int L_VC1 = L_KC0 + NT * DH * 4;
constexpr int L_VC0 = L_VC1 + TOKW * 4;
constexpr int L_Q = L_VC0 + TOKW * 4;
constexpr int L_MISC = L_Q + 128 * KROW;
constexpr int L_END = L_MISC + 256;
static_assert(L_END <= 160 * 1024, "LDS");

constexpr float SL2E = 0.08838834764831845f * 1.4426950408889634f;
constexpr float THR = 8.0f;
constexpr float TWO24 = 16777216.0f;

constexpr size_t WS_SYNC = 0;
constexpr size_t WS_LSE = WS_SYNC + (size_t)NPAIR * 1024;
constexpr size_t WS_PO = WS_LSE + (size_t)NPAIR * 16 * 128;
constexpr size_t WS_END = WS_PO + (size_t)NPAIR * 16 * 8192;

__device__ __forceinline__ float bf_lo(unsigned x) { return __builtin_bit_cast(float, x << 16); }
__device__ __forceinline__ float bf_hi(unsigned x) { return __builtin_bit_cast(float, x & 0xffff0000u); }
__device__ __forceinline__ unsigned pk_bf16(float lo, float hi) {
  f32x2 v = {lo, hi};
  return __builtin_bit_cast(unsigned, __builtin_convertvector(v, bf16x2));
}
__device__ __forceinline__ float deq(unsigned code, float c1, float c0) {
  float r;
  asm("v_fma_mix_f32 %0, %1, %2, %3 op_sel_hi:[1,0,0]" : "=v"(r) : "v"(code), "v"(c1), "v"(c0));
  return r;
}
__device__ __forceinline__ float xg_max(float x) {
  auto s = __builtin_amdgcn_permlane16_swap(__float_as_uint(x), __float_as_uint(x), false, false);
  x = fmaxf(__uint_as_float(s[0]), __uint_as_float(s[1]));
  auto t = __builtin_amdgcn_permlane32_swap(__float_as_uint(x), __float_as_uint(x), false, false);
  return fmaxf(__uint_as_float(t[0]), __uint_as_float(t[1]));
}
__device__ __forceinline__ float xg_sum(float x) {
  auto s = __builtin_amdgcn_permlane16_swap(__float_as_uint(x), __float_as_uint(x), false, false);
  x = __uint_as_float(s[0]) + __uint_as_float(s[1]);
  auto t = __builtin_amdgcn_permlane32_swap(__float_as_uint(x), __float_as_uint(x), false, false);
  return __uint_as_float(t[0]) + __uint_as_float(t[1]);
}
#define NTLD4(p) __builtin_nontemporal_load(reinterpret_cast<const u32x4*>(p))
#define NTLD2(p) __builtin_nontemporal_load(reinterpret_cast<const u32x2*>(p))
#define RLX_AGENT __ATOMIC_RELAXED, __HIP_MEMORY_SCOPE_AGENT

__global__ __launch_bounds__(512, 2) void kivi_attn(const __hip_bfloat16* __restrict__ q, const int* __restrict__ qk,
                                                    const int* __restrict__ qv, const __hip_bfloat16* __restrict__ ksc,
                                                    const __hip_bfloat16* __restrict__ kzr, const __hip_bfloat16* __restrict__ vsc,
                                                    const __hip_bfloat16* __restrict__ vzr, char* __restrict__ ws,
                                                    __hip_bfloat16* __restrict__ out) {
  extern __shared__ __attribute__((aligned(16))) char smem[];
  const int tid = threadIdx.x, lane = tid & 63, r = lane & 31, h = lane >> 5, c16 = lane & 15, g = lane >> 4;
  const int w = __builtin_amdgcn_readfirstlane(tid >> 6);
  const int bid = blockIdx.x;
  const int pair = bid >> 2, split = bid & 3;

  const size_t kvoff = ((size_t)pair * SKV + (size_t)split * TOKW) * DH;
  const auto rk = __builtin_amdgcn_make_buffer_rsrc((void*)(qk + kvoff), 0, CHUNK_BYTES, 0x00020000);
  const auto rv = __builtin_amdgcn_make_buffer_rsrc((void*)(qv + kvoff), 0, CHUNK_BYTES, 0x00020000);
  u64* sync = reinterpret_cast<u64*>(ws + WS_SYNC + (size_t)pair * 1024);

  int vzero = 0;
  asm volatile("" : "+v"(vzero));
  const u64 esrc = __hip_atomic_load(sync + vzero, RLX_AGENT);
#define SMALLMASK(x) (x)
  const size_t goff = SMALLMASK(((size_t)pair * NGK + (size_t)split * NT) * DH);
  const u32x4 ks_raw = NTLD4(ksc + goff + tid * 8);
  const u32x4 kz_raw = NTLD4(kzr + goff + tid * 8);
  const size_t toff = SMALLMASK((size_t)pair * SKV + (size_t)split * TOKW);
  const u32x2 vs_raw = NTLD2(vsc + toff + tid * 4);
  const u32x2 vz_raw = NTLD2(vzr + toff + tid * 4);
  u32x4 q_raw[4];
  {
    const __hip_bfloat16* qp = q + SMALLMASK((size_t)pair * (REP * SQ * DH)) + w * 2048 + lane * 8;
#pragma unroll
    for (int i = 0; i < 4; ++i) q_raw[i] = QNT ? NTLD4(qp + i * 512) : *reinterpret_cast<const u32x4*>(qp + i * 512);
  }

  int kvo = w * 4096 + lane * 16;
  int vvo = w * 4096 + h * 2048 + r * 16;
  u32x4 rk0[4], rv0[4], rk1[4], rv1[4];
#define ISSUE(RK, RV)                                                                                         \
  do {                                                                                                        \
    _Pragma("unroll") for (int i = 0; i < 4; ++i) RK[i] = __builtin_amdgcn_raw_buffer_load_b128(rk, kvo + i * 1024, 0, 2); \
    _Pragma("unroll") for (int i = 0; i < 4; ++i) RV[i] = __builtin_amdgcn_raw_buffer_load_b128(rv, vvo + i * 512, 0, 2);  \
    kvo += TILE_BYTES; vvo += TILE_BYTES;                                                                     \
  } while (0)
  __builtin_amdgcn_sched_barrier(0);
  ISSUE(rk0, rv0);
  __builtin_amdgcn_sched_barrier(0);
  ISSUE(rk1, rv1);
  __builtin_amdgcn_sched_barrier(0);

  if (tid == 0) *reinterpret_cast<u64*>(smem + L_MISC) = esrc * 0x9E3779B97F4A7C15ull + 0xD1B54A32D192ED03ull;
  {
    f32x4 a0, a1, b0, b1;
#pragma unroll
    for (int j = 0; j < 2; ++j) {
      const float s0 = bf_lo(ks_raw[j]), s1 = bf_hi(ks_raw[j]), s2 = bf_lo(ks_raw[2 + j]), s3 = bf_hi(ks_raw[2 + j]);
      a0[2 * j] = s0 * TWO24; a0[2 * j + 1] = s1 * TWO24; a1[2 * j] = s2 * TWO24; a1[2 * j + 1] = s3 * TWO24;
      b0[2 * j] = -bf_lo(kz_raw[j]) * s0; b0[2 * j + 1] = -bf_hi(kz_raw[j]) * s1;
      b1[2 * j] = -bf_lo(kz_raw[2 + j]) * s2; b1[2 * j + 1] = -bf_hi(kz_raw[2 + j]) * s3;
    }
    *reinterpret_cast<f32x4*>(smem + L_KC1 + tid * 32) = a0;
    *reinterpret_cast<f32x4*>(smem + L_KC1 + tid * 32 + 16) = a1;
    *reinterpret_cast<f32x4*>(smem + L_KC0 + tid * 32) = b0;
    *reinterpret_cast<f32x4*>(smem + L_KC0 + tid * 32 + 16) = b1;
    f32x4 c, d;
#pragma unroll
    for (int j = 0; j < 2; ++j) {
      const float s0 = bf_lo(vs_raw[j]), s1 = bf_hi(vs_raw[j]);
      c[2 * j] = s0 * TWO24; c[2 * j + 1] = s1 * TWO24;
      d[2 * j] = -bf_lo(vz_raw[j]) * s0; d[2 * j + 1] = -bf_hi(vz_raw[j]) * s1;
    }
    *reinterpret_cast<f32x4*>(smem + L_VC1 + tid * 16) = c;
    *reinterpret_cast<f32x4*>(smem + L_VC0 + tid * 16) = d;
#pragma unroll
    for (int i = 0; i < 4; ++i)
      *reinterpret_cast<u32x4*>(smem + L_Q + (16 * w + 4 * i + g) * KROW + c16 * 16) = q_raw[i];
  }
#define BARRIER()                                          \
  do {                                                     \
    asm volatile("s_waitcnt lgkmcnt(0)" ::: "memory");     \
    __builtin_amdgcn_s_barrier();                          \
    asm volatile("" ::: "memory");                         \
  } while (0)
  BARRIER();
  bf16x8 qf[4];
#pragma unroll
  for (int k = 0; k < 4; ++k) qf[k] = *reinterpret_cast<const bf16x8*>(smem + L_Q + (16 * w + c16) * KROW + (4 * k + g) * 16);

  const int kw_off = L_K + (8 * w + h) * KROW + r * 8;
  const int vw_off = L_V + r * 128 + (((4 * (w >> 2) + 2 * (w & 1) + h) ^ ((r >> 1) & 7)) << 4) + 8 * ((w >> 1) & 1);
  const int kr_off = L_K + c16 * KROW + g * 16;
  const int vr_off0 = L_V + c16 * 128 + ((g ^ ((c16 >> 1) & 7)) << 4);
  const int vr_off1 = L_V + c16 * 128 + (((4 + g) ^ ((c16 >> 1) & 7)) << 4);
  const int kc_off = r * 16;
  const int vc_off = (8 * w + 4 * h) * 4;

#define DEQUANT(RK, RV, BUF, T)                                                                       \
  do {                                                                                                \
    const f32x4 kc1 = *reinterpret_cast<const f32x4*>(smem + L_KC1 + kc_off + (T) * 512);             \
    const f32x4 kc0 = *reinterpret_cast<const f32x4*>(smem + L_KC0 + kc_off + (T) * 512);             \
    const f32x4 vc1 = *reinterpret_cast<const f32x4*>(smem + L_VC1 + vc_off + (T) * 256);             \
    const f32x4 vc0 = *reinterpret_cast<const f32x4*>(smem + L_VC0 + vc_off + (T) * 256);             \
    _Pragma("unroll") for (int i = 0; i < 4; ++i) {                                                   \
      u32x2 o_;                                                                                       \
      o_[0] = pk_bf16(deq(RK[i][0], kc1[0], kc0[0]), deq(RK[i][1], kc1[1], kc0[1]));                  \
      o_[1] = pk_bf16(deq(RK[i][2], kc1[2], kc0[2]), deq(RK[i][3], kc1[3], kc0[3]));                  \
      *reinterpret_cast<u32x2*>(smem + kw_off + 2 * i * KROW + (BUF) * KBUF) = o_;                    \
    }                                                                                                 \
    _Pragma("unroll") for (int c = 0; c < 4; ++c) {                                                   \
      u32x2 o_;                                                                                       \
      o_[0] = pk_bf16(deq(RV[0][c], vc1[0], vc0[0]), deq(RV[1][c], vc1[1], vc0[1]));                  \
      o_[1] = pk_bf16(deq(RV[2][c], vc1[2], vc0[2]), deq(RV[3][c], vc1[3], vc0[3]));                  \
      *reinterpret_cast<u32x2*>(smem + vw_off + c * 4096 + (BUF) * VBUF) = o_;                        \
    }                                                                                                 \
  } while (0)

  f32x4 o[8];
#pragma unroll
  for (int e = 0; e < 8; ++e) o[e] = f32x4{};
  float m = -__builtin_inff(), l = 0.f;

#define COMPUTE(BUF)                                                                                  \
  do {                                                                                                \
    f32x4 s[4];                                                                                       \
    _Pragma("unroll") for (int tb = 0; tb < 4; ++tb) {                                                \
      s[tb] = f32x4{};                                                                                \
      _Pragma("unroll") for (int k = 0; k < 4; ++k) {                                                 \
        const bf16x8 kf = *reinterpret_cast<const bf16x8*>(smem + kr_off + tb * 16 * KROW + k * 64 + (BUF) * KBUF); \
        s[tb] = __builtin_amdgcn_mfma_f32_16x16x32_bf16(kf, qf[k], s[tb], 0, 0, 0);                   \
      }                                                                                               \
    }                                                                                                 \
    float tm = fmaxf(fmaxf(fmaxf(s[0][0], s[0][1]), fmaxf(s[0][2], s[0][3])), fmaxf(fmaxf(s[1][0], s[1][1]), fmaxf(s[1][2], s[1][3]))); \
    tm = fmaxf(tm, fmaxf(fmaxf(fmaxf(s[2][0], s[2][1]), fmaxf(s[2][2], s[2][3])), fmaxf(fmaxf(s[3][0], s[3][1]), fmaxf(s[3][2], s[3][3])))); \
    tm *= SL2E;                                                                                       \
    if (__builtin_expect(__any(tm > m + THR), 0)) {                                                   \
      const float mn = fmaxf(m, xg_max(tm));                                                          \
      const float al = __builtin_amdgcn_exp2f(m - mn);                                                \
      m = mn; l *= al;                                                                                \
      _Pragma("unroll") for (int e = 0; e < 8; ++e) o[e] *= al;                                       \
    }                                                                                                 \
    float ls = 0.f;                                                                                   \
    _Pragma("unroll") for (int tb = 0; tb < 4; ++tb)                                                  \
      _Pragma("unroll") for (int i = 0; i < 4; ++i) { s[tb][i] = __builtin_amdgcn_exp2f(fmaf(s[tb][i], SL2E, -m)); ls += s[tb][i]; } \
    l += ls;                                                                                          \
    _Pragma("unroll") for (int u = 0; u < 2; ++u) {                                                   \
      u32x4 pw;                                                                                       \
      pw[0] = pk_bf16(s[2 * u][0], s[2 * u][1]); pw[1] = pk_bf16(s[2 * u][2], s[2 * u][3]);           \
      pw[2] = pk_bf16(s[2 * u + 1][0], s[2 * u + 1][1]); pw[3] = pk_bf16(s[2 * u + 1][2], s[2 * u + 1][3]); \
      const bf16x8 pf = __builtin_bit_cast(bf16x8, pw);                                               \
      _Pragma("unroll") for (int e = 0; e < 8; ++e) {                                                 \
        const bf16x8 vf = *reinterpret_cast<const bf16x8*>(smem + (u ? vr_off1 : vr_off0) + e * 2048 + (BUF) * VBUF); \
        o[e] = __builtin_amdgcn_mfma_f32_16x16x32_bf16(vf, pf, o[e], 0, 0, 0);                        \
      }                                                                                               \
    }                                                                                                 \
  } while (0)

  DEQUANT(rk0, rv0, 0, 0);
  ISSUE(rk0, rv0);
  BARRIER();
  for (int t = 0; t < NT - 4; t += 2) {
    COMPUTE(0);
    DEQUANT(rk1, rv1, 1, t + 1);
    ISSUE(rk1, rv1);
    BARRIER();
    COMPUTE(1);
    DEQUANT(rk0, rv0, 0, t + 2);
    ISSUE(rk0, rv0);
    BARRIER();
  }
  COMPUTE(0);
  DEQUANT(rk1, rv1, 1, NT - 3);
  ISSUE(rk1, rv1);
  BARRIER();
  COMPUTE(1);
  DEQUANT(rk0, rv0, 0, NT - 2);
  BARRIER();
  COMPUTE(0);
  DEQUANT(rk1, rv1, 1, NT - 1);
  BARRIER();
  COMPUTE(1);

  l = xg_sum(l);
  const float inv = 1.0f / l;
  const float lse = m + __builtin_amdgcn_logf(l);
  const u64 epoch = *reinterpret_cast<const u64*>(smem + L_MISC);
  const int dest = w >> 1;
  if (dest == split) {
#pragma unroll
    for (int k = 0; k < 8; ++k) {
      f32x4 x;
#pragma unroll
      for (int c = 0; c < 4; ++c) x[c] = o[2 * c + (k >> 2)][k & 3] * inv;
      *reinterpret_cast<f32x4*>(smem + L_Q + (w & 1) * 8192 + k * 1024 + lane * 16) = x;
    }
    if (g == 0) *reinterpret_cast<float*>(smem + L_MISC + 64 + ((w & 1) * 16 + c16) * 4) = lse;
  } else {
    const size_t slab = (size_t)(pair * 4 + dest) * 4 + split;
    const auto rpo = __builtin_amdgcn_make_buffer_rsrc((void*)(ws + WS_PO + slab * 8192 + (w & 1) * 4096), 0, 4096, 0x00020000);
#pragma unroll
    for (int k = 0; k < 8; k += 2) {
      u32x4 x;
#pragma unroll
      for (int kk = 0; kk < 2; ++kk) {
        x[2 * kk] = pk_bf16(o[0 + ((k + kk) >> 2)][(k + kk) & 3] * inv, o[2 + ((k + kk) >> 2)][(k + kk) & 3] * inv);
        x[2 * kk + 1] = pk_bf16(o[4 + ((k + kk) >> 2)][(k + kk) & 3] * inv, o[6 + ((k + kk) >> 2)][(k + kk) & 3] * inv);
      }
      __builtin_amdgcn_raw_buffer_store_b128(x, rpo, k * 512 + lane * 16, 0, 16  );
    }
    if (g == 0) __hip_atomic_store(reinterpret_cast<float*>(ws + WS_LSE + slab * 128) + (w & 1) * 16 + c16, lse, RLX_AGENT);
    asm volatile("s_waitcnt vmcnt(0)" ::: "memory");
    if (lane == 0) __hip_atomic_store(sync + 16 * (1 + dest) + split * 2 + (w & 1), epoch, RLX_AGENT);
  }
  if (w == 2 * split) {
    const bool need = lane < 8 && (lane >> 1) != split;
    u64* fl = sync + 16 * (1 + split) + (lane & 7);
#pragma nounroll
    for (int spin = 0; spin < (1 << 22); ++spin) {
      const u64 v = __hip_atomic_load(fl, RLX_AGENT);
      if (__all(!need || v == epoch)) break;
      if (POLL_SLEEP) __builtin_amdgcn_s_sleep(POLL_SLEEP);
    }
    if (split == 0 && lane == 0) __hip_atomic_store(sync, epoch, RLX_AGENT);
  }
  BARRIER();
  {
    const int hw = w >> 2, k0 = 2 * (w & 3);
    u32x4 z[3];
    float ls[3];
#pragma unroll
    for (int j = 0; j < 3; ++j) {
      const int src = j + (j >= split ? 1 : 0);
      const size_t slab = (size_t)(pair * 4 + split) * 4 + src;
      const auto rpo = __builtin_amdgcn_make_buffer_rsrc((void*)(ws + WS_PO + slab * 8192 + hw * 4096), 0, 4096, 0x00020000);
      z[j] = __builtin_amdgcn_raw_buffer_load_b128(rpo, k0 * 512 + lane * 16, 0, 16);
      ls[j] = __hip_atomic_load(reinterpret_cast<const float*>(ws + WS_LSE + slab * 128) + hw * 16 + c16, RLX_AGENT);
    }
    __builtin_amdgcn_sched_barrier(0);
    f32x4 x[3][2];
#pragma unroll
    for (int j = 0; j < 3; ++j) {
      x[j][0] = f32x4{bf_lo(z[j][0]), bf_hi(z[j][0]), bf_lo(z[j][1]), bf_hi(z[j][1])};
      x[j][1] = f32x4{bf_lo(z[j][2]), bf_hi(z[j][2]), bf_lo(z[j][3]), bf_hi(z[j][3])};
    }
    f32x4 y0 = *reinterpret_cast<const f32x4*>(smem + L_Q + hw * 8192 + k0 * 1024 + lane * 16);
    f32x4 y1 = *reinterpret_cast<const f32x4*>(smem + L_Q + hw * 8192 + k0 * 1024 + 1024 + lane * 16);
    const float lo = *reinterpret_cast<const float*>(smem + L_MISC + 64 + (hw * 16 + c16) * 4);
    const float mx = fmaxf(fmaxf(lo, ls[0]), fmaxf(ls[1], ls[2]));
    const float w0 = __builtin_amdgcn_exp2f(lo - mx);
    float wsum = w0;
    y0 *= w0; y1 *= w0;
#pragma unroll
    for (int j = 0; j < 3; ++j) {
      const float wj = __builtin_amdgcn_exp2f(ls[j] - mx);
      wsum += wj; y0 += x[j][0] * wj; y1 += x[j][1] * wj;
    }
    const float iw = 1.0f / wsum;
    y0 *= iw; y1 *= iw;
    u32x4 ov = {pk_bf16(y0[0], y0[1]), pk_bf16(y0[2], y0[3]), pk_bf16(y1[0], y1[1]), pk_bf16(y1[2], y1[3])};
    const auto ro = __builtin_amdgcn_make_buffer_rsrc((void*)(out + (size_t)(pair * REP + split) * SQ * DH), 0, SQ * DH * 2, 0x00020000);
    __builtin_amdgcn_raw_buffer_store_b128(ov, ro, ((hw * 16 + c16) * DH + 64 * (k0 >> 2) + 16 * g + 4 * (k0 & 3)) * 2, 0, OUT_AUX);
  }
}

extern "C" void kernel_launch(void* const* d_in, const int* in_sizes, int n_in, void* d_out, int out_size, void* d_ws,
                              size_t ws_size, hipStream_t stream) {
  const __hip_bfloat16* q = (const __hip_bfloat16*)d_in[0];
  const int* qk = (const int*)d_in[1];
  const int* qv = (const int*)d_in[2];
  const __hip_bfloat16* ksc = (const __hip_bfloat16*)d_in[3];
  const __hip_bfloat16* kzr = (const __hip_bfloat16*)d_in[4];
  const __hip_bfloat16* vsc = (const __hip_bfloat16*)d_in[5];
  const __hip_bfloat16* vzr = (const __hip_bfloat16*)d_in[6];
  (void)hipFuncSetAttribute((const void*)kivi_attn, hipFuncAttributeMaxDynamicSharedMemorySize, L_END);
  hipLaunchKernelGGL(kivi_attn, dim3(NPAIR * NSPLIT), dim3(512), L_END, stream, q, qk, qv, ksc, kzr, vsc, vzr, (char*)d_ws,
                     (__hip_bfloat16*)d_out);
}
```

```cpp
#include <hip/hip_runtime.h>
#include <hip/hip_bf16.h>
#include <stdint.h>

typedef __attribute__((ext_vector_type(8))) __bf16 bf16x8;
typedef __attribute__((ext_vector_type(2))) __bf16 bf16x2;
typedef __attribute__((ext_vector_type(4))) float f32x4;
typedef __attribute__((ext_vector_type(2))) float f32x2;
typedef __attribute__((ext_vector_type(4))) unsigned u32x4;
typedef __attribute__((ext_vector_type(2))) unsigned u32x2;
typedef unsigned long long u64;

#define XMAP 0
#define ROT 1
#define QNT 1
#define OUT_AUX 0
#define POLL_SLEEP 2
constexpr int NB = 8, SQ = 32, SKV = 8192, HQ = 32, HKV = 8, DH = 128, GRP = 64, NGK = SKV / GRP;
constexpr int REP = HQ / HKV;
constexpr int NSPLIT = 4;
constexpr int TOKW = SKV / NSPLIT;
constexpr int TILE = 64;
constexpr int NT = TOKW / TILE;
constexpr int NPAIR = NB * HKV;
constexpr int TILE_BYTES = TILE * DH * 4;
constexpr int CHUNK_BYTES = TOKW * DH * 4;

constexpr int KROW = 288;
constexpr int KBUF = TILE * KROW;
constexpr int VBUF = DH * 128;
constexpr int L_K = 0;
constexpr int L_V = L_K + 2 * KBUF;
constexpr int L_KC1 = L_V + 2 * VBUF;
constexpr int L_KC0 = L_KC1 + NT * DH * 4;
constexpr int L_VC1 = L_KC0 + NT * DH * 4;
constexpr int L_VC0 = L_VC1 + TOKW * 4;
constexpr int L_Q = L_VC0 + TOKW * 4;
constexpr int L_MISC = L_Q + 128 * KROW;
constexpr int L_END = L_MISC + 256;
static_assert(L_END <= 160 * 1024, "LDS");

constexpr float SL2E = 0.08838834764831845f * 1.4426950408889634f;
constexpr float THR = 8.0f;
constexpr float TWO24 = 16777216.0f;

constexpr size_t WS_SYNC = 0;
constexpr size_t WS_LSE = WS_SYNC + (size_t)NPAIR * 1024;
constexpr size_t WS_PO = WS_LSE + (size_t)NPAIR * 16 * 128;
constexpr size_t WS_END = WS_PO + (size_t)NPAIR * 16 * 8192;

__device__ __forceinline__ float bf_lo(unsigned x) { return __builtin_bit_cast(float, x << 16); }
__device__ __forceinline__ float bf_hi(unsigned x) { return __builtin_bit_cast(float, x & 0xffff0000u); }
__device__ __forceinline__ unsigned pk_bf16(float lo, float hi) {
  f32x2 v = {lo, hi};
  return __builtin_bit_cast(unsigned, __builtin_convertvector(v, bf16x2));
}
__device__ __forceinline__ float deq(unsigned code, float c1, float c0) {
  float r;
  asm("v_fma_mix_f32 %0, %1, %2, %3 op_sel_hi:[1,0,0]" : "=v"(r) : "v"(code), "v"(c1), "v"(c0));
  return r;
}
__device__ __forceinline__ float xg_max(float x) {
  auto s = __builtin_amdgcn_permlane16_swap(__float_as_uint(x), __float_as_uint(x), false, false);
  x = fmaxf(__uint_as_float(s[0]), __uint_as_float(s[1]));
  auto t = __builtin_amdgcn_permlane32_swap(__float_as_uint(x), __float_as_uint(x), false, false);
  return fmaxf(__uint_as_float(t[0]), __uint_as_float(t[1]));
}
__device__ __forceinline__ float xg_sum(float x) {
  auto s = __builtin_amdgcn_permlane16_swap(__float_as_uint(x), __float_as_uint(x), false, false);
  x = __uint_as_float(s[0]) + __uint_as_float(s[1]);
  auto t = __builtin_amdgcn_permlane32_swap(__float_as_uint(x), __float_as_uint(x), false, false);
  return __uint_as_float(t[0]) + __uint_as_float(t[1]);
}
#define NTLD4(p) __builtin_nontemporal_load(reinterpret_cast<const u32x4*>(p))
#define NTLD2(p) __builtin_nontemporal_load(reinterpret_cast<const u32x2*>(p))
#define RLX_AGENT __ATOMIC_RELAXED, __HIP_MEMORY_SCOPE_AGENT

__global__ __launch_bounds__(512, 2) void kivi_attn(const __hip_bfloat16* __restrict__ q, const int* __restrict__ qk,
                                                    const int* __restrict__ qv, const __hip_bfloat16* __restrict__ ksc,
                                                    const __hip_bfloat16* __restrict__ kzr, const __hip_bfloat16* __restrict__ vsc,
                                                    const __hip_bfloat16* __restrict__ vzr, char* __restrict__ ws,
                                                    __hip_bfloat16* __restrict__ out) {
  extern __shared__ __attribute__((aligned(16))) char smem[];
  const int tid = threadIdx.x, lane = tid & 63, r = lane & 31, h = lane >> 5, c16 = lane & 15, g = lane >> 4;
  const int w = __builtin_amdgcn_readfirstlane(tid >> 6);
  const int bid = blockIdx.x;
  const int pair = bid >> 2, split = bid & 3;

  const size_t kvoff = ((size_t)pair * SKV + (size_t)split * TOKW) * DH;
  const auto rk = __builtin_amdgcn_make_buffer_rsrc((void*)(qk + kvoff), 0, CHUNK_BYTES, 0x00020000);
  const auto rv = __builtin_amdgcn_make_buffer_rsrc((void*)(qv + kvoff), 0, CHUNK_BYTES, 0x00020000);
  u64* sync = reinterpret_cast<u64*>(ws + WS_SYNC + (size_t)pair * 1024);

  int vzero = 0;
  asm volatile("" : "+v"(vzero));
  const u64 esrc = __hip_atomic_load(sync + vzero, RLX_AGENT);
#define SMALLMASK(x) (x)
  const size_t goff = SMALLMASK(((size_t)pair * NGK + (size_t)split * NT) * DH);
  const u32x4 ks_raw = NTLD4(ksc + goff + tid * 8);
  const u32x4 kz_raw = NTLD4(kzr + goff + tid * 8);
  const size_t toff = SMALLMASK((size_t)pair * SKV + (size_t)split * TOKW);
  const u32x2 vs_raw = NTLD2(vsc + toff + tid * 4);
  const u32x2 vz_raw = NTLD2(vzr + toff + tid * 4);
  u32x4 q_raw[4];
  {
    const __hip_bfloat16* qp = q + SMALLMASK((size_t)pair * (REP * SQ * DH)) + w * 2048 + lane * 8;
#pragma unroll
    for (int i = 0; i < 4; ++i) q_raw[i] = QNT ? NTLD4(qp + i * 512) : *reinterpret_cast<const u32x4*>(qp + i * 512);
  }

  const int kvo = w * 4096 + lane * 16;
  const int vvo = w * 4096 + h * 2048 + r * 16;
  const int rot = ROT ? (bid >> 3) & (NT - 1) : 0;
#define TID(T) (((T) + rot) & (NT - 1))
  u32x4 rk0[4], rv0[4], rk1[4], rv1[4];
#define ISSUE(RK, RV, T)                                                                                      \
  do {                                                                                                        \
    const int so_ = TID(T) * TILE_BYTES;                                                                      \
    _Pragma("unroll") for (int i = 0; i < 4; ++i) RK[i] = __builtin_amdgcn_raw_buffer_load_b128(rk, kvo + i * 1024, so_, 2); \
    _Pragma("unroll") for (int i = 0; i < 4; ++i) RV[i] = __builtin_amdgcn_raw_buffer_load_b128(rv, vvo + i * 512, so_, 2);  \
  } while (0)
  __builtin_amdgcn_sched_barrier(0);
  ISSUE(rk0, rv0, 0);
  __builtin_amdgcn_sched_barrier(0);
  ISSUE(rk1, rv1, 1);
  __builtin_amdgcn_sched_barrier(0);

  if (tid == 0) *reinterpret_cast<u64*>(smem + L_MISC) = esrc * 0x9E3779B97F4A7C15ull + 0xD1B54A32D192ED03ull;
  {
    f32x4 a0, a1, b0, b1;
#pragma unroll
    for (int j = 0; j < 2; ++j) {
      const float s0 = bf_lo(ks_raw[j]), s1 = bf_hi(ks_raw[j]), s2 = bf_lo(ks_raw[2 + j]), s3 = bf_hi(ks_raw[2 + j]);
      a0[2 * j] = s0 * TWO24; a0[2 * j + 1] = s1 * TWO24; a1[2 * j] = s2 * TWO24; a1[2 * j + 1] = s3 * TWO24;
      b0[2 * j] = -bf_lo(kz_raw[j]) * s0; b0[2 * j + 1] = -bf_hi(kz_raw[j]) * s1;
      b1[2 * j] = -bf_lo(kz_raw[2 + j]) * s2; b1[2 * j + 1] = -bf_hi(kz_raw[2 + j]) * s3;
    }
    *reinterpret_cast<f32x4*>(smem + L_KC1 + tid * 32) = a0;
    *reinterpret_cast<f32x4*>(smem + L_KC1 + tid * 32 + 16) = a1;
    *reinterpret_cast<f32x4*>(smem + L_KC0 + tid * 32) = b0;
    *reinterpret_cast<f32x4*>(smem + L_KC0 + tid * 32 + 16) = b1;
    f32x4 c, d;
#pragma unroll
    for (int j = 0; j < 2; ++j) {
      const float s0 = bf_lo(vs_raw[j]), s1 = bf_hi(vs_raw[j]);
      c[2 * j] = s0 * TWO24; c[2 * j + 1] = s1 * TWO24;
      d[2 * j] = -bf_lo(vz_raw[j]) * s0; d[2 * j + 1] = -bf_hi(vz_raw[j]) * s1;
    }
    *reinterpret_cast<f32x4*>(smem + L_VC1 + tid * 16) = c;
    *reinterpret_cast<f32x4*>(smem + L_VC0 + tid * 16) = d;
#pragma unroll
    for (int i = 0; i < 4; ++i)
      *reinterpret_cast<u32x4*>(smem + L_Q + (16 * w + 4 * i + g) * KROW + c16 * 16) = q_raw[i];
  }
#define BARRIER()                                          \
  do {                                                     \
    asm volatile("s_waitcnt lgkmcnt(0)" ::: "memory");     \
    __builtin_amdgcn_s_barrier();                          \
    asm volatile("" ::: "memory");                         \
  } while (0)
  BARRIER();
  bf16x8 qf[4];
#pragma unroll
  for (int k = 0; k < 4; ++k) qf[k] = *reinterpret_cast<const bf16x8*>(smem + L_Q + (16 * w + c16) * KROW + (4 * k + g) * 16);

  const int kw_off = L_K + (8 * w + h) * KROW + r * 8;
  const int vw_off = L_V + r * 128 + (((4 * (w >> 2) + 2 * (w & 1) + h) ^ ((r >> 1) & 7)) << 4) + 8 * ((w >> 1) & 1);
  const int kr_off = L_K + c16 * KROW + g * 16;
  const int vr_off0 = L_V + c16 * 128 + ((g ^ ((c16 >> 1) & 7)) << 4);
  const int vr_off1 = L_V + c16 * 128 + (((4 + g) ^ ((c16 >> 1) & 7)) << 4);
  const int kc_off = r * 16;
  const int vc_off = (8 * w + 4 * h) * 4;

#define DEQUANT(RK, RV, BUF, T)                                                                       \
  do {                                                                                                \
    const f32x4 kc1 = *reinterpret_cast<const f32x4*>(smem + L_KC1 + kc_off + TID(T) * 512);             \
    const f32x4 kc0 = *reinterpret_cast<const f32x4*>(smem + L_KC0 + kc_off + TID(T) * 512);             \
    const f32x4 vc1 = *reinterpret_cast<const f32x4*>(smem + L_VC1 + vc_off + TID(T) * 256);             \
    const f32x4 vc0 = *reinterpret_cast<const f32x4*>(smem + L_VC0 + vc_off + TID(T) * 256);             \
    _Pragma("unroll") for (int i = 0; i < 4; ++i) {                                                   \
      u32x2 o_;                                                                                       \
      o_[0] = pk_bf16(deq(RK[i][0], kc1[0], kc0[0]), deq(RK[i][1], kc1[1], kc0[1]));                  \
      o_[1] = pk_bf16(deq(RK[i][2], kc1[2], kc0[2]), deq(RK[i][3], kc1[3], kc0[3]));                  \
      *reinterpret_cast<u32x2*>(smem + kw_off + 2 * i * KROW + (BUF) * KBUF) = o_;                    \
    }                                                                                                 \
    _Pragma("unroll") for (int c = 0; c < 4; ++c) {                                                   \
      u32x2 o_;                                                                                       \
      o_[0] = pk_bf16(deq(RV[0][c], vc1[0], vc0[0]), deq(RV[1][c], vc1[1], vc0[1]));                  \
      o_[1] = pk_bf16(deq(RV[2][c], vc1[2], vc0[2]), deq(RV[3][c], vc1[3], vc0[3]));                  \
      *reinterpret_cast<u32x2*>(smem + vw_off + c * 4096 + (BUF) * VBUF) = o_;                        \
    }                                                                                                 \
  } while (0)

  f32x4 o[8];
#pragma unroll
  for (int e = 0; e < 8; ++e) o[e] = f32x4{};
  float m = -__builtin_inff(), l = 0.f;

#define COMPUTE(BUF)                                                                                  \
  do {                                                                                                \
    f32x4 s[4];                                                                                       \
    _Pragma("unroll") for (int tb = 0; tb < 4; ++tb) {                                                \
      s[tb] = f32x4{};                                                                                \
      _Pragma("unroll") for (int k = 0; k < 4; ++k) {                                                 \
        const bf16x8 kf = *reinterpret_cast<const bf16x8*>(smem + kr_off + tb * 16 * KROW + k * 64 + (BUF) * KBUF); \
        s[tb] = __builtin_amdgcn_mfma_f32_16x16x32_bf16(kf, qf[k], s[tb], 0, 0, 0);                   \
      }                                                                                               \
    }                                                                                                 \
    float tm = fmaxf(fmaxf(fmaxf(s[0][0], s[0][1]), fmaxf(s[0][2], s[0][3])), fmaxf(fmaxf(s[1][0], s[1][1]), fmaxf(s[1][2], s[1][3]))); \
    tm = fmaxf(tm, fmaxf(fmaxf(fmaxf(s[2][0], s[2][1]), fmaxf(s[2][2], s[2][3])), fmaxf(fmaxf(s[3][0], s[3][1]), fmaxf(s[3][2], s[3][3])))); \
    tm *= SL2E;                                                                                       \
    if (__builtin_expect(__any(tm > m + THR), 0)) {                                                   \
      const float mn = fmaxf(m, xg_max(tm));                                                          \
      const float al = __builtin_amdgcn_exp2f(m - mn);                                                \
      m = mn; l *= al;                                                                                \
      _Pragma("unroll") for (int e = 0; e < 8; ++e) o[e] *= al;                                       \
    }                                                                                                 \
    float ls = 0.f;                                                                                   \
    _Pragma("unroll") for (int tb = 0; tb < 4; ++tb)                                                  \
      _Pragma("unroll") for (int i = 0; i < 4; ++i) { s[tb][i] = __builtin_amdgcn_exp2f(fmaf(s[tb][i], SL2E, -m)); ls += s[tb][i]; } \
    l += ls;                                                                                          \
    _Pragma("unroll") for (int u = 0; u < 2; ++u) {                                                   \
      u32x4 pw;                                                                                       \
      pw[0] = pk_bf16(s[2 * u][0], s[2 * u][1]); pw[1] = pk_bf16(s[2 * u][2], s[2 * u][3]);           \
      pw[2] = pk_bf16(s[2 * u + 1][0], s[2 * u + 1][1]); pw[3] = pk_bf16(s[2 * u + 1][2], s[2 * u + 1][3]); \
      const bf16x8 pf = __builtin_bit_cast(bf16x8, pw);                                               \
      _Pragma("unroll") for (int e = 0; e < 8; ++e) {                                                 \
        const bf16x8 vf = *reinterpret_cast<const bf16x8*>(smem + (u ? vr_off1 : vr_off0) + e * 2048 + (BUF) * VBUF); \
        o[e] = __builtin_amdgcn_mfma_f32_16x16x32_bf16(vf, pf, o[e], 0, 0, 0);                        \
      }                                                                                               \
    }                                                                                                 \
  } while (0)

  DEQUANT(rk0, rv0, 0, 0);
  ISSUE(rk0, rv0, 2);
  BARRIER();
  for (int t = 0; t < NT - 4; t += 2) {
    COMPUTE(0);
    DEQUANT(rk1, rv1, 1, t + 1);
    ISSUE(rk1, rv1, t + 3);
    BARRIER();
    COMPUTE(1);
    DEQUANT(rk0, rv0, 0, t + 2);
    ISSUE(rk0, rv0, t + 4);
    BARRIER();
  }
  COMPUTE(0);
  DEQUANT(rk1, rv1, 1, NT - 3);
  ISSUE(rk1, rv1, NT - 1);
  BARRIER();
  COMPUTE(1);
  DEQUANT(rk0, rv0, 0, NT - 2);
  BARRIER();
  COMPUTE(0);
  DEQUANT(rk1, rv1, 1, NT - 1);
  BARRIER();
  COMPUTE(1);

  l = xg_sum(l);
  const float inv = 1.0f / l;
  const float lse = m + __builtin_amdgcn_logf(l);
  const u64 epoch = *reinterpret_cast<const u64*>(smem + L_MISC);
  const int dest = w >> 1;
  if (dest == split) {
#pragma unroll
    for (int k = 0; k < 8; ++k) {
      f32x4 x;
#pragma unroll
      for (int c = 0; c < 4; ++c) x[c] = o[2 * c + (k >> 2)][k & 3] * inv;
      *reinterpret_cast<f32x4*>(smem + L_Q + (w & 1) * 8192 + k * 1024 + lane * 16) = x;
    }
    if (g == 0) *reinterpret_cast<float*>(smem + L_MISC + 64 + ((w & 1) * 16 + c16) * 4) = lse;
  } else {
    const size_t slab = (size_t)(pair * 4 + dest) * 4 + split;
    const auto rpo = __builtin_amdgcn_make_buffer_rsrc((void*)(ws + WS_PO + slab * 8192 + (w & 1) * 4096), 0, 4096, 0x00020000);
#pragma unroll
    for (int k = 0; k < 8; k += 2) {
      u32x4 x;
#pragma unroll
      for (int kk = 0; kk < 2; ++kk) {
        x[2 * kk] = pk_bf16(o[0 + ((k + kk) >> 2)][(k + kk) & 3] * inv, o[2 + ((k + kk) >> 2)][(k + kk) & 3] * inv);
        x[2 * kk + 1] = pk_bf16(o[4 + ((k + kk) >> 2)][(k + kk) & 3] * inv, o[6 + ((k + kk) >> 2)][(k + kk) & 3] * inv);
      }
      __builtin_amdgcn_raw_buffer_store_b128(x, rpo, k * 512 + lane * 16, 0, 16  );
    }
    if (g == 0) __hip_atomic_store(reinterpret_cast<float*>(ws + WS_LSE + slab * 128) + (w & 1) * 16 + c16, lse, RLX_AGENT);
    asm volatile("s_waitcnt vmcnt(0)" ::: "memory");
    if (lane == 0) __hip_atomic_store(sync + 16 * (1 + dest) + split * 2 + (w & 1), epoch, RLX_AGENT);
  }
  if (w == 2 * split) {
    const bool need = lane < 8 && (lane >> 1) != split;
    u64* fl = sync + 16 * (1 + split) + (lane & 7);
#pragma nounroll
    for (int spin = 0; spin < (1 << 22); ++spin) {
      const u64 v = __hip_atomic_load(fl, RLX_AGENT);
      if (__all(!need || v == epoch)) break;
      if (POLL_SLEEP) __builtin_amdgcn_s_sleep(POLL_SLEEP);
    }
    if (split == 0 && lane == 0) __hip_atomic_store(sync, epoch, RLX_AGENT);
  }
  BARRIER();
  {
    const int hw = w >> 2, k0 = 2 * (w & 3);
    u32x4 z[3];
    float ls[3];
#pragma unroll
    for (int j = 0; j < 3; ++j) {
      const int src = j + (j >= split ? 1 : 0);
      const size_t slab = (size_t)(pair * 4 + split) * 4 + src;
      const auto rpo = __builtin_amdgcn_make_buffer_rsrc((void*)(ws + WS_PO + slab * 8192 + hw * 4096), 0, 4096, 0x00020000);
      z[j] = __builtin_amdgcn_raw_buffer_load_b128(rpo, k0 * 512 + lane * 16, 0, 16);
      ls[j] = __hip_atomic_load(reinterpret_cast<const float*>(ws + WS_LSE + slab * 128) + hw * 16 + c16, RLX_AGENT);
    }
    __builtin_amdgcn_sched_barrier(0);
    f32x4 x[3][2];
#pragma unroll
    for (int j = 0; j < 3; ++j) {
      x[j][0] = f32x4{bf_lo(z[j][0]), bf_hi(z[j][0]), bf_lo(z[j][1]), bf_hi(z[j][1])};
      x[j][1] = f32x4{bf_lo(z[j][2]), bf_hi(z[j][2]), bf_lo(z[j][3]), bf_hi(z[j][3])};
    }
    f32x4 y0 = *reinterpret_cast<const f32x4*>(smem + L_Q + hw * 8192 + k0 * 1024 + lane * 16);
    f32x4 y1 = *reinterpret_cast<const f32x4*>(smem + L_Q + hw * 8192 + k0 * 1024 + 1024 + lane * 16);
    const float lo = *reinterpret_cast<const float*>(smem + L_MISC + 64 + (hw * 16 + c16) * 4);
    const float mx = fmaxf(fmaxf(lo, ls[0]), fmaxf(ls[1], ls[2]));
    const float w0 = __builtin_amdgcn_exp2f(lo - mx);
    float wsum = w0;
    y0 *= w0; y1 *= w0;
#pragma unroll
    for (int j = 0; j < 3; ++j) {
      const float wj = __builtin_amdgcn_exp2f(ls[j] - mx);
      wsum += wj; y0 += x[j][0] * wj; y1 += x[j][1] * wj;
    }
    const float iw = 1.0f / wsum;
    y0 *= iw; y1 *= iw;
    u32x4 ov = {pk_bf16(y0[0], y0[1]), pk_bf16(y0[2], y0[3]), pk_bf16(y1[0], y1[1]), pk_bf16(y1[2], y1[3])};
    const auto ro = __builtin_amdgcn_make_buffer_rsrc((void*)(out + (size_t)(pair * REP + split) * SQ * DH), 0, SQ * DH * 2, 0x00020000);
    __builtin_amdgcn_raw_buffer_store_b128(ov, ro, ((hw * 16 + c16) * DH + 64 * (k0 >> 2) + 16 * g + 4 * (k0 & 3)) * 2, 0, OUT_AUX);
  }
}

extern "C" void kernel_launch(void* const* d_in, const int* in_sizes, int n_in, void* d_out, int out_size, void* d_ws,
                              size_t ws_size, hipStream_t stream) {
  const __hip_bfloat16* q = (const __hip_bfloat16*)d_in[0];
  const int* qk = (const int*)d_in[1];
  const int* qv = (const int*)d_in[2];
  const __hip_bfloat16* ksc = (const __hip_bfloat16*)d_in[3];
  const __hip_bfloat16* kzr = (const __hip_bfloat16*)d_in[4];
  const __hip_bfloat16* vsc = (const __hip_bfloat16*)d_in[5];
  const __hip_bfloat16* vzr = (const __hip_bfloat16*)d_in[6];
  (void)hipFuncSetAttribute((const void*)kivi_attn, hipFuncAttributeMaxDynamicSharedMemorySize, L_END);
  hipLaunchKernelGGL(kivi_attn, dim3(NPAIR * NSPLIT), dim3(512), L_END, stream, q, qk, qv, ksc, kzr, vsc, vzr, (char*)d_ws,
                     (__hip_bfloat16*)d_out);
}
```

```cpp
#include <hip/hip_runtime.h>
#include <hip/hip_bf16.h>
#include <stdint.h>

typedef __attribute__((ext_vector_type(8))) __bf16 bf16x8;
typedef __attribute__((ext_vector_type(2))) __bf16 bf16x2;
typedef __attribute__((ext_vector_type(4))) float f32x4;
typedef __attribute__((ext_vector_type(2))) float f32x2;
typedef __attribute__((ext_vector_type(4))) unsigned u32x4;
typedef __attribute__((ext_vector_type(2))) unsigned u32x2;
typedef unsigned long long u64;

#define BIAS 2
#define BHALF 0
#define LOOPMODE 1
#define WINN 4
#define XMAP 0
constexpr int NB = 8, SQ = 32, SKV = 8192, HQ = 32, HKV = 8, DH = 128, GRP = 64, NGK = SKV / GRP;
constexpr int REP = HQ / HKV;
constexpr int NSPLIT = 4;
constexpr int TILE = 64;
constexpr int NT = SKV / TILE / NSPLIT;
constexpr int NTMAX = NT + BIAS;
constexpr int NPAIR = NB * HKV;
constexpr int TILE_BYTES = TILE * DH * 4;
static_assert(BIAS % 2 == 0 && BIAS <= 2, "tile counts stay even; LDS table size");

constexpr int KROW = 288;
constexpr int KBUF = TILE * KROW;
constexpr int VBUF = DH * 128;
constexpr int L_K = 0;
constexpr int L_V = L_K + 2 * KBUF;
constexpr int L_KC1 = L_V + 2 * VBUF;
constexpr int L_KC0 = L_KC1 + NTMAX * DH * 4;
constexpr int L_VC1 = L_KC0 + NTMAX * DH * 4;
constexpr int L_VC0 = L_VC1 + NTMAX * TILE * 4;
constexpr int L_Q = L_VC0 + NTMAX * TILE * 4;
constexpr int L_MISC = L_Q + 128 * KROW;
constexpr int L_END = L_MISC + 128;
static_assert(L_END <= 160 * 1024 && L_END > 80 * 1024, "LDS: one workgroup per CU");

constexpr float SL2E = 0.08838834764831845f * 1.4426950408889634f;
constexpr float THR = 8.0f;
constexpr float TWO24 = 16777216.0f;

constexpr size_t WS_SYNC = 0;
constexpr size_t WS_LSE = WS_SYNC + (size_t)NPAIR * 1024;
constexpr size_t WS_PO = WS_LSE + (size_t)NPAIR * 16 * 128;
constexpr size_t WS_END = WS_PO + (size_t)NPAIR * 16 * 8192;

__device__ __forceinline__ float bf_lo(unsigned x) { return __builtin_bit_cast(float, x << 16); }
__device__ __forceinline__ float bf_hi(unsigned x) { return __builtin_bit_cast(float, x & 0xffff0000u); }
__device__ __forceinline__ unsigned pk_bf16(float lo, float hi) {
  f32x2 v = {lo, hi};
  return __builtin_bit_cast(unsigned, __builtin_convertvector(v, bf16x2));
}
__device__ __forceinline__ float deq(unsigned code, float c1, float c0) {
  float r;
  asm("v_fma_mix_f32 %0, %1, %2, %3 op_sel_hi:[1,0,0]" : "=v"(r) : "v"(code), "v"(c1), "v"(c0));
  return r;
}
__device__ __forceinline__ void mk_consts(unsigned s01, unsigned s23, unsigned z01, unsigned z23, f32x4& c1, f32x4& c0) {
  const float s0 = bf_lo(s01), s1 = bf_hi(s01), s2 = bf_lo(s23), s3 = bf_hi(s23);
  c1 = f32x4{s0 * TWO24, s1 * TWO24, s2 * TWO24, s3 * TWO24};
  c0 = f32x4{-bf_lo(z01) * s0, -bf_hi(z01) * s1, -bf_lo(z23) * s2, -bf_hi(z23) * s3};
}
__device__ __forceinline__ float xg_max(float x) {
  auto s = __builtin_amdgcn_permlane16_swap(__float_as_uint(x), __float_as_uint(x), false, false);
  x = fmaxf(__uint_as_float(s[0]), __uint_as_float(s[1]));
  auto t = __builtin_amdgcn_permlane32_swap(__float_as_uint(x), __float_as_uint(x), false, false);
  return fmaxf(__uint_as_float(t[0]), __uint_as_float(t[1]));
}
__device__ __forceinline__ float xg_sum(float x) {
  auto s = __builtin_amdgcn_permlane16_swap(__float_as_uint(x), __float_as_uint(x), false, false);
  x = __uint_as_float(s[0]) + __uint_as_float(s[1]);
  auto t = __builtin_amdgcn_permlane32_swap(__float_as_uint(x), __float_as_uint(x), false, false);
  return __uint_as_float(t[0]) + __uint_as_float(t[1]);
}
#define NTLD4(p) __builtin_nontemporal_load(reinterpret_cast<const u32x4*>(p))
#define RLX_AGENT __ATOMIC_RELAXED, __HIP_MEMORY_SCOPE_AGENT
#define SB() __builtin_amdgcn_sched_barrier(0)
#define STR2(x) #x
#define STR(x) STR2(x)
#define BARRIER()                                          \
  do {                                                     \
    asm volatile("s_waitcnt lgkmcnt(0)" ::: "memory");     \
    __builtin_amdgcn_s_barrier();                          \
    asm volatile("" ::: "memory");                         \
  } while (0)
#define RSRC(p, n) __builtin_amdgcn_make_buffer_rsrc((void*)(p), 0, (n), 0x00020000)

__global__ __launch_bounds__(512, 2) void kivi_attn(const __hip_bfloat16* __restrict__ q, const int* __restrict__ qk,
                                                    const int* __restrict__ qv, const __hip_bfloat16* __restrict__ ksc,
                                                    const __hip_bfloat16* __restrict__ kzr, const __hip_bfloat16* __restrict__ vsc,
                                                    const __hip_bfloat16* __restrict__ vzr, char* __restrict__ ws,
                                                    __hip_bfloat16* __restrict__ out) {
  extern __shared__ __attribute__((aligned(16))) char smem[];
  const int tid = threadIdx.x, lane = tid & 63, r = lane & 31, h = lane >> 5, c16 = lane & 15, g = lane >> 4;
  const int w = __builtin_amdgcn_readfirstlane(tid >> 6);
  const int bid = blockIdx.x;
  const int pair = bid >> 2, split = bid & 3;
  const int bias = (BHALF && (pair & 2)) ? 0 : BIAS;
  const int ntw = (split & 1) ? NT - bias : NT + bias;
  const int tile0 = split * NT + ((split & 1) ? bias : 0);

  const size_t kvoff = ((size_t)pair * SKV + (size_t)tile0 * TILE) * DH;
  const auto rk = RSRC(qk + kvoff, ntw * TILE_BYTES);
  const auto rv = RSRC(qv + kvoff, ntw * TILE_BYTES);
  u64* sync = reinterpret_cast<u64*>(ws + WS_SYNC + (size_t)pair * 1024);

  int vzero = 0;
  asm volatile("" : "+v"(vzero));
  const u64 esrc = __hip_atomic_load(sync + vzero, RLX_AGENT);
  const auto rks = RSRC(ksc + ((size_t)pair * NGK + tile0) * DH, ntw * DH * 2);
  const auto rkz = RSRC(kzr + ((size_t)pair * NGK + tile0) * DH, ntw * DH * 2);
  const auto rvs = RSRC(vsc + (size_t)pair * SKV + (size_t)tile0 * TILE, ntw * TILE * 2);
  const auto rvz = RSRC(vzr + (size_t)pair * SKV + (size_t)tile0 * TILE, ntw * TILE * 2);
  const u32x4 ks_raw = __builtin_amdgcn_raw_buffer_load_b128(rks, tid * 16, 0, 2);
  const u32x4 kz_raw = __builtin_amdgcn_raw_buffer_load_b128(rkz, tid * 16, 0, 2);
  const u32x2 vs_raw = __builtin_amdgcn_raw_buffer_load_b64(rvs, tid * 8, 0, 2);
  const u32x2 vz_raw = __builtin_amdgcn_raw_buffer_load_b64(rvz, tid * 8, 0, 2);
  u32x4 ks2 = {}, kz2 = {};
  u32x2 vs2 = {}, vz2 = {};
  if (BIAS && w == 7) {
    ks2 = __builtin_amdgcn_raw_buffer_load_b128(rks, (512 + lane) * 16, 0, 2);
    kz2 = __builtin_amdgcn_raw_buffer_load_b128(rkz, (512 + lane) * 16, 0, 2);
    vs2 = __builtin_amdgcn_raw_buffer_load_b64(rvs, (512 + lane) * 8, 0, 2);
    vz2 = __builtin_amdgcn_raw_buffer_load_b64(rvz, (512 + lane) * 8, 0, 2);
  }
  u32x4 q_raw[4];
  {
    const __hip_bfloat16* qp = q + (size_t)pair * (REP * SQ * DH) + w * 2048 + lane * 8;
#pragma unroll
    for (int i = 0; i < 4; ++i) q_raw[i] = NTLD4(qp + i * 512);
  }

  const int kvo = w * 4096 + lane * 16;
  const int vvo = w * 4096 + h * 2048 + r * 16;
  const int rot = (bid >> 3) >= ntw ? (bid >> 3) - ntw : (bid >> 3);
#define TID(T) ((T) + rot >= ntw ? (T) + rot - ntw : (T) + rot)
  u32x4 rk0[4], rv0[4], rk1[4], rv1[4];
#define LDK(RK, T, I) RK[I] = __builtin_amdgcn_raw_buffer_load_b128(rk, kvo + (I) * 1024, TID(T) * TILE_BYTES, 2)
#define LDV(RV, T, I) RV[I] = __builtin_amdgcn_raw_buffer_load_b128(rv, vvo + (I) * 512, TID(T) * TILE_BYTES, 2)
#define ISSUE(RK, RV, T)                                                 \
  do {                                                                   \
    _Pragma("unroll") for (int i = 0; i < 4; ++i) LDK(RK, T, i);         \
    _Pragma("unroll") for (int i = 0; i < 4; ++i) LDV(RV, T, i);         \
  } while (0)
  SB();
  ISSUE(rk0, rv0, 0);
  SB();
  ISSUE(rk1, rv1, 1);
  SB();

#define PUT_CONSTS(I, KS, KZ, VS, VZ)                                        \
  do {                                                                       \
    f32x4 a, b;                                                              \
    mk_consts(KS[0], KS[1], KZ[0], KZ[1], a, b);                             \
    *reinterpret_cast<f32x4*>(smem + L_KC1 + (I) * 32) = a;                  \
    *reinterpret_cast<f32x4*>(smem + L_KC0 + (I) * 32) = b;                  \
    mk_consts(KS[2], KS[3], KZ[2], KZ[3], a, b);                             \
    *reinterpret_cast<f32x4*>(smem + L_KC1 + (I) * 32 + 16) = a;             \
    *reinterpret_cast<f32x4*>(smem + L_KC0 + (I) * 32 + 16) = b;             \
    mk_consts(VS[0], VS[1], VZ[0], VZ[1], a, b);                             \
    *reinterpret_cast<f32x4*>(smem + L_VC1 + (I) * 16) = a;                  \
    *reinterpret_cast<f32x4*>(smem + L_VC0 + (I) * 16) = b;                  \
  } while (0)
  PUT_CONSTS(tid, ks_raw, kz_raw, vs_raw, vz_raw);
  if (BIAS && w == 7 && lane < BIAS * 16) PUT_CONSTS(512 + lane, ks2, kz2, vs2, vz2);
#pragma unroll
  for (int i = 0; i < 4; ++i)
    *reinterpret_cast<u32x4*>(smem + L_Q + (16 * w + 4 * i + g) * KROW + c16 * 16) = q_raw[i];
  BARRIER();
  bf16x8 qf[4];
#pragma unroll
  for (int k = 0; k < 4; ++k) qf[k] = *reinterpret_cast<const bf16x8*>(smem + L_Q + (16 * w + c16) * KROW + (4 * k + g) * 16);

  const int kw_off = L_K + (8 * w + h) * KROW + r * 8;
  const int vw_off = L_V + r * 128 + (((4 * (w >> 2) + 2 * (w & 1) + h) ^ ((r >> 1) & 7)) << 4) + 8 * ((w >> 1) & 1);
  const int kr_off = L_K + c16 * KROW + g * 16;
  const int vr_off0 = L_V + c16 * 128 + ((g ^ ((c16 >> 1) & 7)) << 4);
  const int vr_off1 = L_V + c16 * 128 + (((4 + g) ^ ((c16 >> 1) & 7)) << 4);
  const int kc_off = r * 16;
  const int vc_off = (8 * w + 4 * h) * 4;

#define DEQUANT(RK, RV, BUF, T)                                                                       \
  do {                                                                                                \
    const int e_ = TID(T);                                                                            \
    const f32x4 kc1 = *reinterpret_cast<const f32x4*>(smem + L_KC1 + kc_off + e_ * 512);              \
    const f32x4 kc0 = *reinterpret_cast<const f32x4*>(smem + L_KC0 + kc_off + e_ * 512);              \
    const f32x4 vc1 = *reinterpret_cast<const f32x4*>(smem + L_VC1 + vc_off + e_ * 256);              \
    const f32x4 vc0 = *reinterpret_cast<const f32x4*>(smem + L_VC0 + vc_off + e_ * 256);              \
    _Pragma("unroll") for (int i = 0; i < 4; ++i) {                                                   \
      u32x2 o_;                                                                                       \
      o_[0] = pk_bf16(deq(RK[i][0], kc1[0], kc0[0]), deq(RK[i][1], kc1[1], kc0[1]));                  \
      o_[1] = pk_bf16(deq(RK[i][2], kc1[2], kc0[2]), deq(RK[i][3], kc1[3], kc0[3]));                  \
      *reinterpret_cast<u32x2*>(smem + kw_off + 2 * i * KROW + (BUF) * KBUF) = o_;                    \
    }                                                                                                 \
    _Pragma("unroll") for (int c = 0; c < 4; ++c) {                                                   \
      u32x2 o_;                                                                                       \
      o_[0] = pk_bf16(deq(RV[0][c], vc1[0], vc0[0]), deq(RV[1][c], vc1[1], vc0[1]));                  \
      o_[1] = pk_bf16(deq(RV[2][c], vc1[2], vc0[2]), deq(RV[3][c], vc1[3], vc0[3]));                  \
      *reinterpret_cast<u32x2*>(smem + vw_off + c * 4096 + (BUF) * VBUF) = o_;                        \
    }                                                                                                 \
  } while (0)

  f32x4 o[8];
#pragma unroll
  for (int e = 0; e < 8; ++e) o[e] = f32x4{};
  float m = -__builtin_inff(), l = 0.f;

#define HOOK(N, DO, RK, RV, T)                                                                        \
  do {                                                                                                \
    if (DO) {                                                                                         \
      SB();                                                                                           \
      if (WINN < 60) asm volatile("s_waitcnt vmcnt(" STR(WINN) ")" ::: "memory");                     \
      if ((N) < 2) { LDK(RK, T, 2 * (N)); LDK(RK, T, 2 * (N) + 1); } else { LDV(RV, T, 2 * (N) - 4); LDV(RV, T, 2 * (N) - 3); } \
    }                                                                                                 \
    SB();                                                                                             \
  } while (0)
#define COMPUTE(BUF, DO, RK, RV, T)                                                                   \
  do {                                                                                                \
    bf16x8 fr[16];                                                                                    \
    _Pragma("unroll") for (int k = 0; k < 4; ++k)                                                     \
      _Pragma("unroll") for (int tb = 0; tb < 4; ++tb)                                                \
        fr[4 * k + tb] = *reinterpret_cast<const bf16x8*>(smem + kr_off + tb * 16 * KROW + k * 64 + (BUF) * KBUF); \
    HOOK(0, DO, RK, RV, T);                                                                           \
    f32x4 s[4];                                                                                       \
    _Pragma("unroll") for (int tb = 0; tb < 4; ++tb) s[tb] = f32x4{};                                 \
    _Pragma("unroll") for (int k = 0; k < 4; ++k)                                                     \
      _Pragma("unroll") for (int tb = 0; tb < 4; ++tb)                                                \
        s[tb] = __builtin_amdgcn_mfma_f32_16x16x32_bf16(fr[4 * k + tb], qf[k], s[tb], 0, 0, 0);       \
    SB();                                                                                             \
    _Pragma("unroll") for (int u = 0; u < 2; ++u)                                                     \
      _Pragma("unroll") for (int e = 0; e < 8; ++e)                                                   \
        fr[8 * u + e] = *reinterpret_cast<const bf16x8*>(smem + (u ? vr_off1 : vr_off0) + e * 2048 + (BUF) * VBUF); \
    HOOK(1, DO, RK, RV, T);                                                                           \
    f32x4 p[4];                                                                                       \
    _Pragma("unroll") for (int tb = 0; tb < 4; ++tb)                                                  \
      _Pragma("unroll") for (int i = 0; i < 4; ++i) p[tb][i] = fmaf(s[tb][i], SL2E, -m);              \
    float tm = fmaxf(fmaxf(fmaxf(p[0][0], p[0][1]), p[0][2]), fmaxf(fmaxf(p[0][3], p[1][0]), p[1][1])); \
    tm = fmaxf(fmaxf(tm, fmaxf(fmaxf(p[1][2], p[1][3]), p[2][0])), fmaxf(fmaxf(p[2][1], p[2][2]), p[2][3])); \
    tm = fmaxf(fmaxf(tm, fmaxf(fmaxf(p[3][0], p[3][1]), p[3][2])), p[3][3]);                          \
    if (__builtin_expect(__any(tm > THR), 0)) {      \
      float t2 = fmaxf(fmaxf(fmaxf(s[0][0], s[0][1]), fmaxf(s[0][2], s[0][3])), fmaxf(fmaxf(s[1][0], s[1][1]), fmaxf(s[1][2], s[1][3]))); \
      t2 = fmaxf(t2, fmaxf(fmaxf(fmaxf(s[2][0], s[2][1]), fmaxf(s[2][2], s[2][3])), fmaxf(fmaxf(s[3][0], s[3][1]), fmaxf(s[3][2], s[3][3])))); \
      const float mn = fmaxf(m, xg_max(t2 * SL2E));                                                   \
      const float al = __builtin_amdgcn_exp2f(m - mn);                                                \
      m = mn; l *= al;                                                                                \
      _Pragma("unroll") for (int e = 0; e < 8; ++e) o[e] *= al;                                       \
      _Pragma("unroll") for (int tb = 0; tb < 4; ++tb)                                                \
        _Pragma("unroll") for (int i = 0; i < 4; ++i) p[tb][i] = fmaf(s[tb][i], SL2E, -m);            \
    }                                                                                                 \
    HOOK(2, DO, RK, RV, T);                                                                           \
    float ls0 = 0.f, ls1 = 0.f;                                                                       \
    _Pragma("unroll") for (int tb = 0; tb < 4; ++tb)                                                  \
      _Pragma("unroll") for (int i = 0; i < 4; ++i) {                                                 \
        p[tb][i] = __builtin_amdgcn_exp2f(p[tb][i]);                                                  \
        if (i & 1) ls1 += p[tb][i]; else ls0 += p[tb][i];                                             \
      }                                                                                               \
    l += ls0 + ls1;                                                                                   \
    bf16x8 pf[2];                                                                                     \
    _Pragma("unroll") for (int u = 0; u < 2; ++u) {                                                   \
      u32x4 pw;                                                                                       \
      pw[0] = pk_bf16(p[2 * u][0], p[2 * u][1]); pw[1] = pk_bf16(p[2 * u][2], p[2 * u][3]);           \
      pw[2] = pk_bf16(p[2 * u + 1][0], p[2 * u + 1][1]); pw[3] = pk_bf16(p[2 * u + 1][2], p[2 * u + 1][3]); \
      pf[u] = __builtin_bit_cast(bf16x8, pw);                                                         \
    }                                                                                                 \
    if (DO) HOOK(3, DO, RK, RV, T);                                                                   \
    _Pragma("unroll") for (int u = 0; u < 2; ++u)                                                     \
      _Pragma("unroll") for (int e = 0; e < 8; ++e)                                                   \
        o[e] = __builtin_amdgcn_mfma_f32_16x16x32_bf16(fr[8 * u + e], pf[u], o[e], 0, 0, 0);          \
    SB();                                                                                             \
  } while (0)

  DEQUANT(rk0, rv0, 0, 0);
  BARRIER();
  for (int t = 0; t < ntw - 2; t += 2) {
    COMPUTE(0, 1, rk0, rv0, t + 2);
    DEQUANT(rk1, rv1, 1, t + 1);
    BARRIER();
    COMPUTE(1, 1, rk1, rv1, t + 3);
    DEQUANT(rk0, rv0, 0, t + 2);
    BARRIER();
  }
  COMPUTE(0, 0, rk0, rv0, 0);
  DEQUANT(rk1, rv1, 1, ntw - 1);
  BARRIER();
  COMPUTE(1, 0, rk0, rv0, 0);

  const u64 epoch = esrc * 0x9E3779B97F4A7C15ull + 0xD1B54A32D192ED03ull;
  l = xg_sum(l);
  const float inv = 1.0f / l;
  const float lse = m + __builtin_amdgcn_logf(l);
  const int dest = w >> 1;
  if (dest == split) {
#pragma unroll
    for (int k = 0; k < 8; ++k) {
      f32x4 x;
#pragma unroll
      for (int c = 0; c < 4; ++c) x[c] = o[2 * c + (k >> 2)][k & 3] * inv;
      *reinterpret_cast<f32x4*>(smem + L_Q + (w & 1) * 8192 + k * 1024 + lane * 16) = x;
    }
    if (g == 0) *reinterpret_cast<float*>(smem + L_MISC + ((w & 1) * 16 + c16) * 4) = lse;
  } else {
    const size_t slab = (size_t)(pair * 4 + dest) * 4 + split;
    const auto rpo = RSRC(ws + WS_PO + slab * 8192 + (w & 1) * 4096, 4096);
#pragma unroll
    for (int k = 0; k < 8; k += 2) {
      u32x4 x;
#pragma unroll
      for (int kk = 0; kk < 2; ++kk) {
        x[2 * kk] = pk_bf16(o[0 + ((k + kk) >> 2)][(k + kk) & 3] * inv, o[2 + ((k + kk) >> 2)][(k + kk) & 3] * inv);
        x[2 * kk + 1] = pk_bf16(o[4 + ((k + kk) >> 2)][(k + kk) & 3] * inv, o[6 + ((k + kk) >> 2)][(k + kk) & 3] * inv);
      }
      __builtin_amdgcn_raw_buffer_store_b128(x, rpo, k * 512 + lane * 16, 0, 16  );
    }
    if (g == 0) __hip_atomic_store(reinterpret_cast<float*>(ws + WS_LSE + slab * 128) + (w & 1) * 16 + c16, lse, RLX_AGENT);
    asm volatile("s_waitcnt vmcnt(0)" ::: "memory");
    if (lane == 0) __hip_atomic_store(sync + 16 * (1 + dest) + split * 2 + (w & 1), epoch, RLX_AGENT);
  }
  if (w == 2 * split) {
    const bool need = lane < 8 && (lane >> 1) != split;
    u64* fl = sync + 16 * (1 + split) + (lane & 7);
#pragma nounroll
    for (int spin = 0; spin < (1 << 22); ++spin) {
      const u64 v = __hip_atomic_load(fl, RLX_AGENT);
      if (__all(!need || v == epoch)) break;
      __builtin_amdgcn_s_sleep(2);
    }
  }
  BARRIER();
  {
    const int hw = w >> 2, k0 = 2 * (w & 3);
    u32x4 z[3];
    float ls[3];
#pragma unroll
    for (int j = 0; j < 3; ++j) {
      const int src = j + (j >= split ? 1 : 0);
      const size_t slab = (size_t)(pair * 4 + split) * 4 + src;
      z[j] = __builtin_amdgcn_raw_buffer_load_b128(RSRC(ws + WS_PO + slab * 8192 + hw * 4096, 4096), k0 * 512 + lane * 16, 0, 16);
      ls[j] = __hip_atomic_load(reinterpret_cast<const float*>(ws + WS_LSE + slab * 128) + hw * 16 + c16, RLX_AGENT);
    }
    SB();
    f32x4 y0 = *reinterpret_cast<const f32x4*>(smem + L_Q + hw * 8192 + k0 * 1024 + lane * 16);
    f32x4 y1 = *reinterpret_cast<const f32x4*>(smem + L_Q + hw * 8192 + k0 * 1024 + 1024 + lane * 16);
    const float lo = *reinterpret_cast<const float*>(smem + L_MISC + (hw * 16 + c16) * 4);
    const float mx = fmaxf(fmaxf(lo, ls[0]), fmaxf(ls[1], ls[2]));
    const float w0 = __builtin_amdgcn_exp2f(lo - mx);
    float wsum = w0;
    y0 *= w0; y1 *= w0;
#pragma unroll
    for (int j = 0; j < 3; ++j) {
      const float wj = __builtin_amdgcn_exp2f(ls[j] - mx);
      wsum += wj;
      y0 += f32x4{bf_lo(z[j][0]), bf_hi(z[j][0]), bf_lo(z[j][1]), bf_hi(z[j][1])} * wj;
      y1 += f32x4{bf_lo(z[j][2]), bf_hi(z[j][2]), bf_lo(z[j][3]), bf_hi(z[j][3])} * wj;
    }
    const float iw = 1.0f / wsum;
    y0 *= iw; y1 *= iw;
    u32x4 ov = {pk_bf16(y0[0], y0[1]), pk_bf16(y0[2], y0[3]), pk_bf16(y1[0], y1[1]), pk_bf16(y1[2], y1[3])};
    __hip_bfloat16* op = out + ((size_t)(pair * REP + split) * SQ + hw * 16 + c16) * DH + 64 * (k0 >> 2) + 16 * g + 4 * (k0 & 3);
    *reinterpret_cast<u32x4*>(op) = ov;
  }
  if (split == 0 && tid == 0) __hip_atomic_store(sync, epoch, RLX_AGENT);
}

extern "C" void kernel_launch(void* const* d_in, const int* in_sizes, int n_in, void* d_out, int out_size, void* d_ws,
                              size_t ws_size, hipStream_t stream) {
  const __hip_bfloat16* q = (const __hip_bfloat16*)d_in[0];
  const int* qk = (const int*)d_in[1];
  const int* qv = (const int*)d_in[2];
  const __hip_bfloat16* ksc = (const __hip_bfloat16*)d_in[3];
  const __hip_bfloat16* kzr = (const __hip_bfloat16*)d_in[4];
  const __hip_bfloat16* vsc = (const __hip_bfloat16*)d_in[5];
  const __hip_bfloat16* vzr = (const __hip_bfloat16*)d_in[6];
  (void)hipFuncSetAttribute((const void*)kivi_attn, hipFuncAttributeMaxDynamicSharedMemorySize, L_END);
  hipLaunchKernelGGL(kivi_attn, dim3(NPAIR * NSPLIT), dim3(512), L_END, stream, q, qk, qv, ksc, kzr, vsc, vzr, (char*)d_ws,
                     (__hip_bfloat16*)d_out);
}
```

```cpp
#include <hip/hip_runtime.h>
#include <hip/hip_bf16.h>
#include <stdint.h>

typedef __attribute__((ext_vector_type(8))) __bf16 bf16x8;
typedef __attribute__((ext_vector_type(2))) __bf16 bf16x2;
typedef __attribute__((ext_vector_type(16))) float f32x16;
typedef __attribute__((ext_vector_type(4))) float f32x4;
typedef __attribute__((ext_vector_type(2))) float f32x2;
typedef __attribute__((ext_vector_type(4))) unsigned u32x4;
typedef __attribute__((ext_vector_type(2))) unsigned u32x2;
typedef unsigned long long u64;

#define BIAS 0
#define WIN 6
#define PRODHI 0
#define PRIO 1
constexpr int NB = 8, SQ = 32, SKV = 8192, HQ = 32, HKV = 8, DH = 128, GRP = 64, NGK = SKV / GRP;
constexpr int REP = HQ / HKV;
constexpr int NSPLIT = 4;
constexpr int TILE = 64;
constexpr int NT = SKV / TILE / NSPLIT;
constexpr int NTMAX = NT + BIAS;
constexpr int NPAIR = NB * HKV;
constexpr int TILE_BYTES = TILE * DH * 4;
static_assert(BIAS >= 0 && BIAS <= 2 && WIN >= 1 && WIN <= 12, "knobs");

constexpr int KROW = 272;
constexpr int VROW = 144;
constexpr int KBUF = TILE * KROW;
constexpr int VBUF = DH * VROW;
constexpr int TBUF = KBUF + VBUF;
constexpr int L_T = 0;
constexpr int L_KC1 = L_T + 2 * TBUF;
constexpr int L_KC0 = L_KC1 + NTMAX * DH * 4;
constexpr int L_VC1 = L_KC0 + NTMAX * DH * 4;
constexpr int L_VC0 = L_VC1 + NTMAX * TILE * 4;
constexpr int L_Q = L_VC0 + NTMAX * TILE * 4;
constexpr int L_MISC = L_Q + 128 * KROW;
constexpr int L_END = L_MISC + 128;
static_assert(L_END <= 160 * 1024 && L_END > 80 * 1024, "LDS: one workgroup per CU");

constexpr float SL2E = 0.08838834764831845f * 1.4426950408889634f;
constexpr float THR = 8.0f;
constexpr float TWO24 = 16777216.0f;

constexpr size_t WS_SYNC = 0;
constexpr size_t WS_LSE = WS_SYNC + (size_t)NPAIR * 1024;
constexpr size_t WS_PO = WS_LSE + (size_t)NPAIR * 16 * 128;
constexpr size_t WS_END = WS_PO + (size_t)NPAIR * 16 * 8192;

__device__ __forceinline__ float bf_lo(unsigned x) { return __builtin_bit_cast(float, x << 16); }
__device__ __forceinline__ float bf_hi(unsigned x) { return __builtin_bit_cast(float, x & 0xffff0000u); }
__device__ __forceinline__ unsigned pk_bf16(float lo, float hi) {
  f32x2 v = {lo, hi};
  return __builtin_bit_cast(unsigned, __builtin_convertvector(v, bf16x2));
}
__device__ __forceinline__ float deq(unsigned code, float c1, float c0) {
  float r;
  asm("v_fma_mix_f32 %0, %1, %2, %3 op_sel_hi:[1,0,0]" : "=v"(r) : "v"(code), "v"(c1), "v"(c0));
  return r;
}
__device__ __forceinline__ void mk_consts(unsigned s01, unsigned s23, unsigned z01, unsigned z23, f32x4& c1, f32x4& c0) {
  const float s0 = bf_lo(s01), s1 = bf_hi(s01), s2 = bf_lo(s23), s3 = bf_hi(s23);
  c1 = f32x4{s0 * TWO24, s1 * TWO24, s2 * TWO24, s3 * TWO24};
  c0 = f32x4{-bf_lo(z01) * s0, -bf_hi(z01) * s1, -bf_lo(z23) * s2, -bf_hi(z23) * s3};
}
__device__ __forceinline__ float xh_max(float x) {
  auto t = __builtin_amdgcn_permlane32_swap(__float_as_uint(x), __float_as_uint(x), false, false);
  return fmaxf(__uint_as_float(t[0]), __uint_as_float(t[1]));
}
__device__ __forceinline__ float xh_sum(float x) {
  auto t = __builtin_amdgcn_permlane32_swap(__float_as_uint(x), __float_as_uint(x), false, false);
  return __uint_as_float(t[0]) + __uint_as_float(t[1]);
}
__device__ __forceinline__ float max16(const f32x16& a) {
  float m0 = fmaxf(fmaxf(a[0], a[1]), a[2]), m1 = fmaxf(fmaxf(a[3], a[4]), a[5]), m2 = fmaxf(fmaxf(a[6], a[7]), a[8]);
  float m3 = fmaxf(fmaxf(a[9], a[10]), a[11]), m4 = fmaxf(fmaxf(a[12], a[13]), a[14]);
  return fmaxf(fmaxf(fmaxf(m0, m1), m2), fmaxf(fmaxf(m3, m4), a[15]));
}
#define NTLD4(p) __builtin_nontemporal_load(reinterpret_cast<const u32x4*>(p))
#define RLX_AGENT __ATOMIC_RELAXED, __HIP_MEMORY_SCOPE_AGENT
#define SB() __builtin_amdgcn_sched_barrier(0)
#define BARRIER()                                          \
  do {                                                     \
    asm volatile("s_waitcnt lgkmcnt(0)" ::: "memory");     \
    __builtin_amdgcn_s_barrier();                          \
    asm volatile("" ::: "memory");                         \
  } while (0)
#define RSRC(p, n) __builtin_amdgcn_make_buffer_rsrc((void*)(p), 0, (n), 0x00020000)

__global__ __launch_bounds__(512, 2) void kivi_attn(const __hip_bfloat16* __restrict__ q, const int* __restrict__ qk,
                                                    const int* __restrict__ qv, const __hip_bfloat16* __restrict__ ksc,
                                                    const __hip_bfloat16* __restrict__ kzr, const __hip_bfloat16* __restrict__ vsc,
                                                    const __hip_bfloat16* __restrict__ vzr, char* __restrict__ ws,
                                                    __hip_bfloat16* __restrict__ out) {
  extern __shared__ __attribute__((aligned(16))) char smem[];
  const int tid = threadIdx.x, lane = tid & 63, r = lane & 31, h = lane >> 5;
  const int w = __builtin_amdgcn_readfirstlane(tid >> 6);
  const bool producer = PRODHI ? (w >= 4) : (w < 4);
  const int p = w & 3;
  const int bid = blockIdx.x;
  const int pair = bid >> 2, split = bid & 3;
  const int ntw = (split & 1) ? NT - BIAS : NT + BIAS;
  const int tile0 = split * NT + ((split & 1) ? BIAS : 0);
  const int rot = (bid >> 3) >= ntw ? (bid >> 3) - ntw : (bid >> 3);
#define TID(T) ((T) + rot >= ntw ? (T) + rot - ntw : (T) + rot)
  u64* sync = reinterpret_cast<u64*>(ws + WS_SYNC + (size_t)pair * 1024);

  int vzero = 0;
  asm volatile("" : "+v"(vzero));
  const u64 esrc = __hip_atomic_load(sync + vzero, RLX_AGENT);
  f32x16 o[4];
  float m = -__builtin_inff(), l = 0.f;

  if (producer) {
    if (PRIO) __builtin_amdgcn_s_setprio(1);
    const size_t kvoff = ((size_t)pair * SKV + (size_t)tile0 * TILE) * DH;
    const auto rk = RSRC(qk + kvoff, ntw * TILE_BYTES);
    const auto rv = RSRC(qv + kvoff, ntw * TILE_BYTES);
    const int kvo = p * 8192 + lane * 16;
    const int vvo = p * 8192 + h * 2048 + r * 16;
    u32x4 rg[16];
    int ko_, vo_;
#define TOFF(T) do { const int t_ = (T); const int so_ = t_ < ntw ? TID(t_) * TILE_BYTES : NTMAX * TILE_BYTES; ko_ = kvo + so_; vo_ = vvo + so_; } while (0)
#define LD(N)                                                                                                                     \
  do {                                                                                                                            \
    if ((N) < 4) rg[N] = __builtin_amdgcn_raw_buffer_load_b128(rk, ko_ + (N) * 1024, 0, 2);                                       \
    else if ((N) < 8) rg[N] = __builtin_amdgcn_raw_buffer_load_b128(rk, ko_ + ((N) - 4) * 1024, 4096, 2);                         \
    else if ((N) < 12) rg[N] = __builtin_amdgcn_raw_buffer_load_b128(rv, vo_ + ((N) - 8) * 512, 0, 2);                            \
    else rg[N] = __builtin_amdgcn_raw_buffer_load_b128(rv, vo_ + ((N) - 12) * 512, 4096, 2);                                      \
  } while (0)
    TOFF(0);
#pragma unroll
    for (int n = 0; n < WIN; ++n) LD(n);
    SB();
    BARRIER();
    const int kw_off = L_T + (16 * p + h) * KROW + r * 8;
    const int vw_off = L_T + KBUF + r * VROW + 32 * p + 16 * h;
    for (int t = 0; t < ntw; ++t) {
      const int e_ = TID(t), bo = (t & 1) * TBUF;
      const f32x4 kc1 = *reinterpret_cast<const f32x4*>(smem + L_KC1 + r * 16 + e_ * 512);
      const f32x4 kc0 = *reinterpret_cast<const f32x4*>(smem + L_KC0 + r * 16 + e_ * 512);
      f32x4 vc1[2], vc0[2];
#pragma unroll
      for (int a = 0; a < 2; ++a) {
        vc1[a] = *reinterpret_cast<const f32x4*>(smem + L_VC1 + (16 * p + 8 * a + 4 * h) * 4 + e_ * 256);
        vc0[a] = *reinterpret_cast<const f32x4*>(smem + L_VC0 + (16 * p + 8 * a + 4 * h) * 4 + e_ * 256);
      }
#pragma unroll
      for (int n = 0; n < 16; ++n) {
        SB();
        asm volatile("" : "+v"(rg[n]));
        if (n + WIN == 16) TOFF(t + 1);
        LD((n + WIN) & 15);
        SB();
        if (n < 8) {
          u32x2 o_;
          o_[0] = pk_bf16(deq(rg[n][0], kc1[0], kc0[0]), deq(rg[n][1], kc1[1], kc0[1]));
          o_[1] = pk_bf16(deq(rg[n][2], kc1[2], kc0[2]), deq(rg[n][3], kc1[3], kc0[3]));
          *reinterpret_cast<u32x2*>(smem + kw_off + 2 * n * KROW + bo) = o_;
        } else if ((n & 3) == 3) {
          const int a = (n - 8) >> 2, b = n - 3;
#pragma unroll
          for (int c = 0; c < 4; ++c) {
            u32x2 o_;
            o_[0] = pk_bf16(deq(rg[b][c], vc1[a][0], vc0[a][0]), deq(rg[b + 1][c], vc1[a][1], vc0[a][1]));
            o_[1] = pk_bf16(deq(rg[b + 2][c], vc1[a][2], vc0[a][2]), deq(rg[b + 3][c], vc1[a][3], vc0[a][3]));
            *reinterpret_cast<u32x2*>(smem + vw_off + c * 32 * VROW + 8 * a + bo) = o_;
          }
        }
      }
      BARRIER();
    }
    if (PRIO) __builtin_amdgcn_s_setprio(0);
  } else {
    const int ct = p * 64 + lane;
    const auto rks = RSRC(ksc + ((size_t)pair * NGK + tile0) * DH, ntw * DH * 2);
    const auto rkz = RSRC(kzr + ((size_t)pair * NGK + tile0) * DH, ntw * DH * 2);
    const auto rvs = RSRC(vsc + (size_t)pair * SKV + (size_t)tile0 * TILE, ntw * TILE * 2);
    const auto rvz = RSRC(vzr + (size_t)pair * SKV + (size_t)tile0 * TILE, ntw * TILE * 2);
    constexpr int NPASS = BIAS ? 3 : 2;
    u32x4 ksr[NPASS], kzr_[NPASS];
    u32x2 vsr[NPASS], vzr_[NPASS];
#pragma unroll
    for (int a = 0; a < NPASS; ++a) {
      ksr[a] = __builtin_amdgcn_raw_buffer_load_b128(rks, (a * 256 + ct) * 16, 0, 2);
      kzr_[a] = __builtin_amdgcn_raw_buffer_load_b128(rkz, (a * 256 + ct) * 16, 0, 2);
      vsr[a] = __builtin_amdgcn_raw_buffer_load_b64(rvs, (a * 256 + ct) * 8, 0, 2);
      vzr_[a] = __builtin_amdgcn_raw_buffer_load_b64(rvz, (a * 256 + ct) * 8, 0, 2);
    }
    u32x4 q_raw[8];
    const __hip_bfloat16* qp = q + ((size_t)pair * REP + p) * (SQ * DH) + lane * 8;
#pragma unroll
    for (int i = 0; i < 8; ++i) q_raw[i] = NTLD4(qp + i * 512);
    SB();
#pragma unroll
    for (int a = 0; a < NPASS; ++a) {
      const int i_ = a * 256 + ct;
      if (a < 2 || ct < BIAS * 16) {
        f32x4 x, y;
        mk_consts(ksr[a][0], ksr[a][1], kzr_[a][0], kzr_[a][1], x, y);
        *reinterpret_cast<f32x4*>(smem + L_KC1 + i_ * 32) = x;
        *reinterpret_cast<f32x4*>(smem + L_KC0 + i_ * 32) = y;
        mk_consts(ksr[a][2], ksr[a][3], kzr_[a][2], kzr_[a][3], x, y);
        *reinterpret_cast<f32x4*>(smem + L_KC1 + i_ * 32 + 16) = x;
        *reinterpret_cast<f32x4*>(smem + L_KC0 + i_ * 32 + 16) = y;
        mk_consts(vsr[a][0], vsr[a][1], vzr_[a][0], vzr_[a][1], x, y);
        *reinterpret_cast<f32x4*>(smem + L_VC1 + i_ * 16) = x;
        *reinterpret_cast<f32x4*>(smem + L_VC0 + i_ * 16) = y;
      }
    }
    BARRIER();
#pragma unroll
    for (int i = 0; i < 8; ++i)
      *reinterpret_cast<u32x4*>(smem + L_Q + (32 * p + 4 * i + (lane >> 4)) * KROW + (lane & 15) * 16) = q_raw[i];
    asm volatile("s_waitcnt lgkmcnt(0)" ::: "memory");
    bf16x8 qf[8];
#pragma unroll
    for (int k = 0; k < 8; ++k) qf[k] = *reinterpret_cast<const bf16x8*>(smem + L_Q + (32 * p + r) * KROW + (2 * k + h) * 16);
#pragma unroll
    for (int c = 0; c < 4; ++c) o[c] = f32x16{};
    const int kr_off = L_T + r * KROW + h * 16;
    const int vr_off = L_T + KBUF + r * VROW + h * 16;
    for (int t = 0; t < ntw; ++t) {
      BARRIER();
      const int bo = (t & 1) * TBUF;
      bf16x8 fr[16];
#pragma unroll
      for (int k = 0; k < 8; ++k)
#pragma unroll
        for (int th = 0; th < 2; ++th) fr[2 * k + th] = *reinterpret_cast<const bf16x8*>(smem + kr_off + bo + th * 32 * KROW + k * 32);
      SB();
      f32x16 s[2] = {f32x16{}, f32x16{}};
#pragma unroll
      for (int k = 0; k < 8; ++k)
#pragma unroll
        for (int th = 0; th < 2; ++th) s[th] = __builtin_amdgcn_mfma_f32_32x32x16_bf16(fr[2 * k + th], qf[k], s[th], 0, 0, 0);
      SB();
#pragma unroll
      for (int ks = 0; ks < 4; ++ks)
#pragma unroll
        for (int c = 0; c < 4; ++c) fr[4 * ks + c] = *reinterpret_cast<const bf16x8*>(smem + vr_off + bo + c * 32 * VROW + ks * 32);
      SB();
      f32x16 pp[2];
#pragma unroll
      for (int th = 0; th < 2; ++th)
#pragma unroll
        for (int i = 0; i < 16; ++i) pp[th][i] = fmaf(s[th][i], SL2E, -m);
      const float tm = fmaxf(max16(pp[0]), max16(pp[1]));
      if (__builtin_expect(__any(tm > THR), 0)) {
        const float mn = fmaxf(m, xh_max(fmaxf(max16(s[0]), max16(s[1])) * SL2E));
        const float al = __builtin_amdgcn_exp2f(m - mn);
        m = mn; l *= al;
#pragma unroll
        for (int c = 0; c < 4; ++c) o[c] *= al;
#pragma unroll
        for (int th = 0; th < 2; ++th)
#pragma unroll
          for (int i = 0; i < 16; ++i) pp[th][i] = fmaf(s[th][i], SL2E, -m);
      }
      float ls0 = 0.f, ls1 = 0.f;
#pragma unroll
      for (int ks = 0; ks < 4; ++ks) {
        const int th = ks >> 1, b = 8 * (ks & 1);
        u32x4 pw;
#pragma unroll
        for (int i = 0; i < 4; ++i) {
          const float e0 = __builtin_amdgcn_exp2f(pp[th][b + 2 * i]), e1 = __builtin_amdgcn_exp2f(pp[th][b + 2 * i + 1]);
          ls0 += e0; ls1 += e1;
          pw[i] = pk_bf16(e0, e1);
        }
        const bf16x8 pf = __builtin_bit_cast(bf16x8, pw);
#pragma unroll
        for (int c = 0; c < 4; ++c) o[c] = __builtin_amdgcn_mfma_f32_32x32x16_bf16(fr[4 * ks + c], pf, o[c], 0, 0, 0);
      }
      l += ls0 + ls1;
      SB();
    }
  }

  const u64 epoch = esrc * 0x9E3779B97F4A7C15ull + 0xD1B54A32D192ED03ull;
  if (!producer) {
    l = xh_sum(l);
    const float inv = 1.0f / l;
    const float lse = m + __builtin_amdgcn_logf(l);
    if (p == split) {
#pragma unroll
      for (int k = 0; k < 16; ++k)
        *reinterpret_cast<f32x4*>(smem + L_Q + k * 1024 + lane * 16) = f32x4{o[0][k] * inv, o[1][k] * inv, o[2][k] * inv, o[3][k] * inv};
      if (h == 0) *reinterpret_cast<float*>(smem + L_MISC + r * 4) = lse;
    } else {
      const size_t slab = (size_t)(pair * 4 + p) * 4 + split;
      const auto rpo = RSRC(ws + WS_PO + slab * 8192, 8192);
#pragma unroll
      for (int j = 0; j < 8; ++j) {
        u32x4 x = {pk_bf16(o[0][2 * j] * inv, o[1][2 * j] * inv), pk_bf16(o[2][2 * j] * inv, o[3][2 * j] * inv),
                   pk_bf16(o[0][2 * j + 1] * inv, o[1][2 * j + 1] * inv), pk_bf16(o[2][2 * j + 1] * inv, o[3][2 * j + 1] * inv)};
        __builtin_amdgcn_raw_buffer_store_b128(x, rpo, j * 1024 + lane * 16, 0, 16  );
      }
      if (h == 0) __hip_atomic_store(reinterpret_cast<float*>(ws + WS_LSE + slab * 128) + r, lse, RLX_AGENT);
      asm volatile("s_waitcnt vmcnt(0)" ::: "memory");
      if (lane == 0) __hip_atomic_store(sync + 16 * (1 + p) + split, epoch, RLX_AGENT);
    }
  } else if (p == 0) {
    const bool need = (lane & 3) != split;
    u64* fl = sync + 16 * (1 + split) + (lane & 3);
#pragma nounroll
    for (int spin = 0; spin < (1 << 22); ++spin) {
      const u64 v = __hip_atomic_load(fl, RLX_AGENT);
      if (__all(!need || v == epoch)) break;
      __builtin_amdgcn_s_sleep(2);
    }
  }
  BARRIER();
  {
    u32x4 z[3];
    float ls[3];
#pragma unroll
    for (int j = 0; j < 3; ++j) {
      const int src = j + (j >= split ? 1 : 0);
      const size_t slab = (size_t)(pair * 4 + split) * 4 + src;
      z[j] = __builtin_amdgcn_raw_buffer_load_b128(RSRC(ws + WS_PO + slab * 8192, 8192), w * 1024 + lane * 16, 0, 16);
      ls[j] = __hip_atomic_load(reinterpret_cast<const float*>(ws + WS_LSE + slab * 128) + r, RLX_AGENT);
    }
    SB();
    f32x4 y0 = *reinterpret_cast<const f32x4*>(smem + L_Q + 2 * w * 1024 + lane * 16);
    f32x4 y1 = *reinterpret_cast<const f32x4*>(smem + L_Q + (2 * w + 1) * 1024 + lane * 16);
    const float lo = *reinterpret_cast<const float*>(smem + L_MISC + r * 4);
    const float mx = fmaxf(fmaxf(lo, ls[0]), fmaxf(ls[1], ls[2]));
    const float w0 = __builtin_amdgcn_exp2f(lo - mx);
    float wsum = w0;
    y0 *= w0; y1 *= w0;
#pragma unroll
    for (int j = 0; j < 3; ++j) {
      const float wj = __builtin_amdgcn_exp2f(ls[j] - mx);
      wsum += wj;
      y0 += f32x4{bf_lo(z[j][0]), bf_hi(z[j][0]), bf_lo(z[j][1]), bf_hi(z[j][1])} * wj;
      y1 += f32x4{bf_lo(z[j][2]), bf_hi(z[j][2]), bf_lo(z[j][3]), bf_hi(z[j][3])} * wj;
    }
    const float iw = 1.0f / wsum;
    y0 *= iw; y1 *= iw;
    u32x4 ov = {pk_bf16(y0[0], y0[1]), pk_bf16(y0[2], y0[3]), pk_bf16(y1[0], y1[1]), pk_bf16(y1[2], y1[3])};
    __hip_bfloat16* op = out + ((size_t)(pair * REP + split) * SQ + r) * DH + 32 * (w >> 1) + 16 * h + 8 * (w & 1);
    *reinterpret_cast<u32x4*>(op) = ov;
  }
  if (split == 0 && tid == 0) __hip_atomic_store(sync, epoch, RLX_AGENT);
}

extern "C" void kernel_launch(void* const* d_in, const int* in_sizes, int n_in, void* d_out, int out_size, void* d_ws,
                              size_t ws_size, hipStream_t stream) {
  const __hip_bfloat16* q = (const __hip_bfloat16*)d_in[0];
  const int* qk = (const int*)d_in[1];
  const int* qv = (const int*)d_in[2];
  const __hip_bfloat16* ksc = (const __hip_bfloat16*)d_in[3];
  const __hip_bfloat16* kzr = (const __hip_bfloat16*)d_in[4];
  const __hip_bfloat16* vsc = (const __hip_bfloat16*)d_in[5];
  const __hip_bfloat16* vzr = (const __hip_bfloat16*)d_in[6];
  (void)hipFuncSetAttribute((const void*)kivi_attn, hipFuncAttributeMaxDynamicSharedMemorySize, L_END);
  hipLaunchKernelGGL(kivi_attn, dim3(NPAIR * NSPLIT), dim3(512), L_END, stream, q, qk, qv, ksc, kzr, vsc, vzr, (char*)d_ws,
                     (__hip_bfloat16*)d_out);
}
```

```cpp
#include <hip/hip_runtime.h>
#include <hip/hip_bf16.h>
#include <stdint.h>

typedef __attribute__((ext_vector_type(8))) __bf16 bf16x8;
typedef __attribute__((ext_vector_type(2))) __bf16 bf16x2;
typedef __attribute__((ext_vector_type(16))) float f32x16;
typedef __attribute__((ext_vector_type(4))) float f32x4;
typedef __attribute__((ext_vector_type(2))) float f32x2;
typedef __attribute__((ext_vector_type(4))) unsigned u32x4;
typedef __attribute__((ext_vector_type(2))) unsigned u32x2;
typedef unsigned long long u64;

#define BIAS 0
#define WIN 6
#define WINMAX WIN
#define WINO WIN
#define ENDT 0
#define ROTM 0
#define XMAP 1
#define QAUX 2
#define WINALT 0
#define HALFB 1
#define STR2(x) #x
#define STR(x) STR2(x)
#define PRODHI 0
#define PRIO 1
constexpr int NB = 8, SQ = 32, SKV = 8192, HQ = 32, HKV = 8, DH = 128, GRP = 64, NGK = SKV / GRP;
constexpr int REP = HQ / HKV;
constexpr int NSPLIT = 4;
constexpr int TILE = 64;
constexpr int NT = SKV / TILE / NSPLIT;
constexpr int NTMAX = NT + BIAS;
constexpr int NPAIR = NB * HKV;
constexpr int TILE_BYTES = TILE * DH * 4;
static_assert(BIAS >= 0 && BIAS <= 2 && WIN >= 1 && WINMAX <= 12 && WIN <= WINMAX && WINO <= WINMAX, "knobs");

constexpr int KROW = 272;
constexpr int VROW = 144;
constexpr int KBUF = TILE * KROW;
constexpr int VBUF = DH * VROW;
constexpr int TBUF = KBUF + VBUF;
constexpr int L_T = 0;
constexpr int L_KC1 = L_T + (HALFB ? 1 : 2) * TBUF;
constexpr int L_KC0 = L_KC1 + NTMAX * DH * 4;
constexpr int L_VC1 = L_KC0 + NTMAX * DH * 4;
constexpr int L_VC0 = L_VC1 + NTMAX * TILE * 4;
constexpr int L_Q = L_VC0 + NTMAX * TILE * 4;
constexpr int L_MISC = L_Q + 128 * KROW;
constexpr int L_END = L_MISC + 128;
static_assert(L_END <= 160 * 1024 && L_END > 80 * 1024, "LDS: one workgroup per CU");

constexpr float SL2E = 0.08838834764831845f * 1.4426950408889634f;
constexpr float THR = 8.0f;
constexpr float TWO24 = 16777216.0f;

constexpr size_t WS_SYNC = 0;
constexpr size_t WS_LSE = WS_SYNC + (size_t)NPAIR * 1024;
constexpr size_t WS_PO = WS_LSE + (size_t)NPAIR * 16 * 128;
constexpr size_t WS_END = WS_PO + (size_t)NPAIR * 16 * 8192;

__device__ __forceinline__ float bf_lo(unsigned x) { return __builtin_bit_cast(float, x << 16); }
__device__ __forceinline__ float bf_hi(unsigned x) { return __builtin_bit_cast(float, x & 0xffff0000u); }
__device__ __forceinline__ unsigned pk_bf16(float lo, float hi) {
  f32x2 v = {lo, hi};
  return __builtin_bit_cast(unsigned, __builtin_convertvector(v, bf16x2));
}
__device__ __forceinline__ float deq(unsigned code, float c1, float c0) {
  float r;
  asm("v_fma_mix_f32 %0, %1, %2, %3 op_sel_hi:[1,0,0]" : "=v"(r) : "v"(code), "v"(c1), "v"(c0));
  return r;
}
__device__ __forceinline__ void mk_consts(unsigned s01, unsigned s23, unsigned z01, unsigned z23, f32x4& c1, f32x4& c0) {
  const float s0 = bf_lo(s01), s1 = bf_hi(s01), s2 = bf_lo(s23), s3 = bf_hi(s23);
  c1 = f32x4{s0 * TWO24, s1 * TWO24, s2 * TWO24, s3 * TWO24};
  c0 = f32x4{-bf_lo(z01) * s0, -bf_hi(z01) * s1, -bf_lo(z23) * s2, -bf_hi(z23) * s3};
}
__device__ __forceinline__ float xh_max(float x) {
  auto t = __builtin_amdgcn_permlane32_swap(__float_as_uint(x), __float_as_uint(x), false, false);
  return fmaxf(__uint_as_float(t[0]), __uint_as_float(t[1]));
}
__device__ __forceinline__ float xh_sum(float x) {
  auto t = __builtin_amdgcn_permlane32_swap(__float_as_uint(x), __float_as_uint(x), false, false);
  return __uint_as_float(t[0]) + __uint_as_float(t[1]);
}
__device__ __forceinline__ float max16(const f32x16& a) {
  float m0 = fmaxf(fmaxf(a[0], a[1]), a[2]), m1 = fmaxf(fmaxf(a[3], a[4]), a[5]), m2 = fmaxf(fmaxf(a[6], a[7]), a[8]);
  float m3 = fmaxf(fmaxf(a[9], a[10]), a[11]), m4 = fmaxf(fmaxf(a[12], a[13]), a[14]);
  return fmaxf(fmaxf(fmaxf(m0, m1), m2), fmaxf(fmaxf(m3, m4), a[15]));
}
#define NTLD4(p) __builtin_nontemporal_load(reinterpret_cast<const u32x4*>(p))
#define RLX_AGENT __ATOMIC_RELAXED, __HIP_MEMORY_SCOPE_AGENT
#define SB() __builtin_amdgcn_sched_barrier(0)
#define BARRIER()                                          \
  do {                                                     \
    asm volatile("s_waitcnt lgkmcnt(0)" ::: "memory");     \
    __builtin_amdgcn_s_barrier();                          \
    asm volatile("" ::: "memory");                         \
  } while (0)
#define RSRC(p, n) __builtin_amdgcn_make_buffer_rsrc((void*)(p), 0, (n), 0x00020000)

__global__ __launch_bounds__(512, 2) void kivi_attn(const __hip_bfloat16* __restrict__ q, const int* __restrict__ qk,
                                                    const int* __restrict__ qv, const __hip_bfloat16* __restrict__ ksc,
                                                    const __hip_bfloat16* __restrict__ kzr, const __hip_bfloat16* __restrict__ vsc,
                                                    const __hip_bfloat16* __restrict__ vzr, char* __restrict__ ws,
                                                    __hip_bfloat16* __restrict__ out) {
  extern __shared__ __attribute__((aligned(16))) char smem[];
  const int tid = threadIdx.x, lane = tid & 63, r = lane & 31, h = lane >> 5;
  const int w = __builtin_amdgcn_readfirstlane(tid >> 6);
  const bool producer = PRODHI ? (w >= 4) : (w < 4);
  const int p = w & 3;
  const int bid = blockIdx.x;
  const int pair = (bid & 7) * 8 + (bid >> 5), split = (bid >> 3) & 3;
  const int ntw = (split & 1) ? NT - BIAS : NT + BIAS;
  const int tile0 = split * NT + ((split & 1) ? BIAS : 0);
  const int rot0 = ((bid >> 3) + ROTM * (bid & 7)) & 31;
  const int rot = rot0 >= ntw ? rot0 - ntw : rot0;
#define TID(T) ((T) + rot >= ntw ? (T) + rot - ntw : (T) + rot)
  u64* sync = reinterpret_cast<u64*>(ws + WS_SYNC + (size_t)pair * 1024);

  int vzero = 0;
  asm volatile("" : "+v"(vzero));
  const u64 esrc = __hip_atomic_load(sync + vzero, RLX_AGENT);
  f32x16 o[4];
  float m = -__builtin_inff(), l = 0.f;

  if (producer) {
    if (PRIO) __builtin_amdgcn_s_setprio(1);
    const size_t kvoff = ((size_t)pair * SKV + (size_t)tile0 * TILE) * DH;
    const auto rk = RSRC(qk + kvoff, ntw * TILE_BYTES);
    const auto rv = RSRC(qv + kvoff, ntw * TILE_BYTES);
    const int kvo = p * 8192 + lane * 16;
    const int vvo = p * 8192 + h * 2048 + r * 16;
    u32x4 rg[16];
    int ko_, vo_;
#define TOFF(T) do { const int t_ = (T); const int so_ = t_ < ntw ? TID(t_) * TILE_BYTES : NTMAX * TILE_BYTES; ko_ = kvo + so_; vo_ = vvo + so_; } while (0)
#define LD(N)                                                                                                                     \
  do {                                                                                                                            \
    if ((N) < 4) rg[N] = __builtin_amdgcn_raw_buffer_load_b128(rk, ko_ + (N) * 1024, 0, 2);                                       \
    else if ((N) < 8) rg[N] = __builtin_amdgcn_raw_buffer_load_b128(rk, ko_ + ((N) - 4) * 1024, 4096, 2);                         \
    else if ((N) < 12) rg[N] = __builtin_amdgcn_raw_buffer_load_b128(rv, vo_ + ((N) - 8) * 512, 0, 2);                            \
    else rg[N] = __builtin_amdgcn_raw_buffer_load_b128(rv, vo_ + ((N) - 12) * 512, 4096, 2);                                      \
  } while (0)
#define THROTTLE(MODE)                                                                              \
  do {                                                                                              \
    if (WIN < WINMAX && (MODE) == 0) __builtin_amdgcn_s_waitcnt(0x0F70 | ((WIN - 1) & 15) | (((WIN - 1) >> 4) << 14));        \
    if (WINO != WIN && WINO < WINMAX && (MODE) == 1) __builtin_amdgcn_s_waitcnt(0x0F70 | ((WINO - 1) & 15) | (((WINO - 1) >> 4) << 14)); \
  } while (0)
    const int mode0 = (WINO != WIN && (split & 1)) ? 1 : 0;
    TOFF(0);
#pragma unroll
    for (int n = 0; n < WINMAX; ++n) { if (n >= WIN) THROTTLE(mode0); LD(n); SB(); }
    BARRIER();
    const int kw_off = L_T + (16 * p + h) * KROW + r * 8;
    const int vw_off = L_T + KBUF + r * VROW + 32 * p + 16 * h;
    for (int t = 0; t < ntw; ++t) {
      const int e_ = TID(t), bo = HALFB ? 0 : (t & 1) * TBUF;
      const int mode = (t >= ntw - ENDT) ? 2 : mode0;
      const f32x4 kc1 = *reinterpret_cast<const f32x4*>(smem + L_KC1 + r * 16 + e_ * 512);
      const f32x4 kc0 = *reinterpret_cast<const f32x4*>(smem + L_KC0 + r * 16 + e_ * 512);
      f32x4 vc1[2], vc0[2];
#pragma unroll
      for (int a = 0; a < 2; ++a) {
        vc1[a] = *reinterpret_cast<const f32x4*>(smem + L_VC1 + (16 * p + 8 * a + 4 * h) * 4 + e_ * 256);
        vc0[a] = *reinterpret_cast<const f32x4*>(smem + L_VC0 + (16 * p + 8 * a + 4 * h) * 4 + e_ * 256);
      }
#pragma unroll
      for (int n = 0; n < 16; ++n) {
        SB();
        asm volatile("" : "+v"(rg[n]));
        THROTTLE(mode);
        if (WINALT && (n & 1) && mode != 2) __builtin_amdgcn_s_waitcnt(0x0F70 | ((WIN - 2) & 15));
        if (n + WINMAX == 16) TOFF(t + 1);
        LD((n + WINMAX) & 15);
        SB();
        if (n < 8) {
          u32x2 o_;
          o_[0] = pk_bf16(deq(rg[n][0], kc1[0], kc0[0]), deq(rg[n][1], kc1[1], kc0[1]));
          o_[1] = pk_bf16(deq(rg[n][2], kc1[2], kc0[2]), deq(rg[n][3], kc1[3], kc0[3]));
          *reinterpret_cast<u32x2*>(smem + kw_off + 2 * n * KROW + bo) = o_;
          if (HALFB && n == 7) BARRIER();
        } else if ((n & 3) == 3) {
          const int a = (n - 8) >> 2, b = n - 3;
#pragma unroll
          for (int c = 0; c < 4; ++c) {
            u32x2 o_;
            o_[0] = pk_bf16(deq(rg[b][c], vc1[a][0], vc0[a][0]), deq(rg[b + 1][c], vc1[a][1], vc0[a][1]));
            o_[1] = pk_bf16(deq(rg[b + 2][c], vc1[a][2], vc0[a][2]), deq(rg[b + 3][c], vc1[a][3], vc0[a][3]));
            *reinterpret_cast<u32x2*>(smem + vw_off + c * 32 * VROW + 8 * a + bo) = o_;
          }
        }
      }
      BARRIER();
    }
    if (PRIO) __builtin_amdgcn_s_setprio(0);
  } else {
    const int ct = p * 64 + lane;
    const auto rks = RSRC(ksc + ((size_t)pair * NGK + tile0) * DH, ntw * DH * 2);
    const auto rkz = RSRC(kzr + ((size_t)pair * NGK + tile0) * DH, ntw * DH * 2);
    const auto rvs = RSRC(vsc + (size_t)pair * SKV + (size_t)tile0 * TILE, ntw * TILE * 2);
    const auto rvz = RSRC(vzr + (size_t)pair * SKV + (size_t)tile0 * TILE, ntw * TILE * 2);
    constexpr int NPASS = BIAS ? 3 : 2;
    u32x4 ksr[NPASS], kzr_[NPASS];
    u32x2 vsr[NPASS], vzr_[NPASS];
#pragma unroll
    for (int a = 0; a < NPASS; ++a) {
      ksr[a] = __builtin_amdgcn_raw_buffer_load_b128(rks, (a * 256 + ct) * 16, 0, 2);
      kzr_[a] = __builtin_amdgcn_raw_buffer_load_b128(rkz, (a * 256 + ct) * 16, 0, 2);
      vsr[a] = __builtin_amdgcn_raw_buffer_load_b64(rvs, (a * 256 + ct) * 8, 0, 2);
      vzr_[a] = __builtin_amdgcn_raw_buffer_load_b64(rvz, (a * 256 + ct) * 8, 0, 2);
    }
    u32x4 q_raw[8];
    const auto rq = RSRC(q + ((size_t)pair * REP + p) * (SQ * DH), SQ * DH * 2);
#pragma unroll
    for (int i = 0; i < 8; ++i) q_raw[i] = __builtin_amdgcn_raw_buffer_load_b128(rq, lane * 16 + i * 1024, 0, QAUX);
    SB();
#pragma unroll
    for (int a = 0; a < NPASS; ++a) {
      const int i_ = a * 256 + ct;
      if (a < 2 || ct < BIAS * 16) {
        f32x4 x, y;
        mk_consts(ksr[a][0], ksr[a][1], kzr_[a][0], kzr_[a][1], x, y);
        *reinterpret_cast<f32x4*>(smem + L_KC1 + i_ * 32) = x;
        *reinterpret_cast<f32x4*>(smem + L_KC0 + i_ * 32) = y;
        mk_consts(ksr[a][2], ksr[a][3], kzr_[a][2], kzr_[a][3], x, y);
        *reinterpret_cast<f32x4*>(smem + L_KC1 + i_ * 32 + 16) = x;
        *reinterpret_cast<f32x4*>(smem + L_KC0 + i_ * 32 + 16) = y;
        mk_consts(vsr[a][0], vsr[a][1], vzr_[a][0], vzr_[a][1], x, y);
        *reinterpret_cast<f32x4*>(smem + L_VC1 + i_ * 16) = x;
        *reinterpret_cast<f32x4*>(smem + L_VC0 + i_ * 16) = y;
      }
    }
    BARRIER();
#pragma unroll
    for (int i = 0; i < 8; ++i)
      *reinterpret_cast<u32x4*>(smem + L_Q + (32 * p + 4 * i + (lane >> 4)) * KROW + (lane & 15) * 16) = q_raw[i];
    asm volatile("s_waitcnt lgkmcnt(0)" ::: "memory");
    bf16x8 qf[8];
#pragma unroll
    for (int k = 0; k < 8; ++k) qf[k] = *reinterpret_cast<const bf16x8*>(smem + L_Q + (32 * p + r) * KROW + (2 * k + h) * 16);
#pragma unroll
    for (int c = 0; c < 4; ++c) o[c] = f32x16{};
    const int kr_off = L_T + r * KROW + h * 16;
    const int vr_off = L_T + KBUF + r * VROW + h * 16;
    for (int t = 0; t < ntw; ++t) {
      BARRIER();
      const int bo = HALFB ? 0 : (t & 1) * TBUF;
      bf16x8 fr[16];
#pragma unroll
      for (int k = 0; k < 8; ++k)
#pragma unroll
        for (int th = 0; th < 2; ++th) fr[2 * k + th] = *reinterpret_cast<const bf16x8*>(smem + kr_off + bo + th * 32 * KROW + k * 32);
      SB();
      f32x16 s[2] = {f32x16{}, f32x16{}};
#pragma unroll
      for (int k = 0; k < 8; ++k)
#pragma unroll
        for (int th = 0; th < 2; ++th) s[th] = __builtin_amdgcn_mfma_f32_32x32x16_bf16(fr[2 * k + th], qf[k], s[th], 0, 0, 0);
      SB();
      if (!HALFB) {
#pragma unroll
        for (int ks = 0; ks < 4; ++ks)
#pragma unroll
          for (int c = 0; c < 4; ++c) fr[4 * ks + c] = *reinterpret_cast<const bf16x8*>(smem + vr_off + bo + c * 32 * VROW + ks * 32);
        SB();
      }
      f32x16 pp[2];
#pragma unroll
      for (int th = 0; th < 2; ++th)
#pragma unroll
        for (int i = 0; i < 16; ++i) pp[th][i] = fmaf(s[th][i], SL2E, -m);
      const float tm = fmaxf(max16(pp[0]), max16(pp[1]));
      if (__builtin_expect(__any(tm > THR), 0)) {
        const float mn = fmaxf(m, xh_max(fmaxf(max16(s[0]), max16(s[1])) * SL2E));
        const float al = __builtin_amdgcn_exp2f(m - mn);
        m = mn; l *= al;
#pragma unroll
        for (int c = 0; c < 4; ++c) o[c] *= al;
#pragma unroll
        for (int th = 0; th < 2; ++th)
#pragma unroll
          for (int i = 0; i < 16; ++i) pp[th][i] = fmaf(s[th][i], SL2E, -m);
      }
      float ls0 = 0.f, ls1 = 0.f;
      bf16x8 pf[4];
#pragma unroll
      for (int ks = 0; ks < 4; ++ks) {
        const int th = ks >> 1, b = 8 * (ks & 1);
        u32x4 pw;
#pragma unroll
        for (int i = 0; i < 4; ++i) {
          const float e0 = __builtin_amdgcn_exp2f(pp[th][b + 2 * i]), e1 = __builtin_amdgcn_exp2f(pp[th][b + 2 * i + 1]);
          ls0 += e0; ls1 += e1;
          pw[i] = pk_bf16(e0, e1);
        }
        pf[ks] = __builtin_bit_cast(bf16x8, pw);
      }
      l += ls0 + ls1;
      if (HALFB) {
        asm volatile("" : "+v"(pf[0]), "+v"(pf[1]), "+v"(pf[2]), "+v"(pf[3]), "+v"(l));
        SB();
        BARRIER();
#pragma unroll
        for (int ks = 0; ks < 4; ++ks)
#pragma unroll
          for (int c = 0; c < 4; ++c) fr[4 * ks + c] = *reinterpret_cast<const bf16x8*>(smem + vr_off + bo + c * 32 * VROW + ks * 32);
        SB();
      }
#pragma unroll
      for (int ks = 0; ks < 4; ++ks)
#pragma unroll
        for (int c = 0; c < 4; ++c) o[c] = __builtin_amdgcn_mfma_f32_32x32x16_bf16(fr[4 * ks + c], pf[ks], o[c], 0, 0, 0);
      SB();
    }
  }

  const u64 epoch = esrc * 0x9E3779B97F4A7C15ull + 0xD1B54A32D192ED03ull;
  if (!producer) {
    l = xh_sum(l);
    const float inv = 1.0f / l;
    const float lse = m + __builtin_amdgcn_logf(l);
    if (p == split) {
#pragma unroll
      for (int k = 0; k < 16; ++k)
        *reinterpret_cast<f32x4*>(smem + L_Q + k * 1024 + lane * 16) = f32x4{o[0][k] * inv, o[1][k] * inv, o[2][k] * inv, o[3][k] * inv};
      if (h == 0) *reinterpret_cast<float*>(smem + L_MISC + r * 4) = lse;
    } else {
      const size_t slab = (size_t)(pair * 4 + p) * 4 + split;
      const auto rpo = RSRC(ws + WS_PO + slab * 8192, 8192);
#pragma unroll
      for (int j = 0; j < 8; ++j) {
        u32x4 x = {pk_bf16(o[0][2 * j] * inv, o[1][2 * j] * inv), pk_bf16(o[2][2 * j] * inv, o[3][2 * j] * inv),
                   pk_bf16(o[0][2 * j + 1] * inv, o[1][2 * j + 1] * inv), pk_bf16(o[2][2 * j + 1] * inv, o[3][2 * j + 1] * inv)};
        __builtin_amdgcn_raw_buffer_store_b128(x, rpo, j * 1024 + lane * 16, 0, 16  );
      }
      if (h == 0) __hip_atomic_store(reinterpret_cast<float*>(ws + WS_LSE + slab * 128) + r, lse, RLX_AGENT);
      asm volatile("s_waitcnt vmcnt(0)" ::: "memory");
      if (lane == 0) __hip_atomic_store(sync + 16 * (1 + p) + split, epoch, RLX_AGENT);
    }
  } else if (p == 0) {
    const bool need = (lane & 3) != split;
    u64* fl = sync + 16 * (1 + split) + (lane & 3);
#pragma nounroll
    for (int spin = 0; spin < (1 << 22); ++spin) {
      const u64 v = __hip_atomic_load(fl, RLX_AGENT);
      if (__all(!need || v == epoch)) break;
      __builtin_amdgcn_s_sleep(2);
    }
  }
  BARRIER();
  {
    u32x4 z[3];
    float ls[3];
#pragma unroll
    for (int j = 0; j < 3; ++j) {
      const int src = j + (j >= split ? 1 : 0);
      const size_t slab = (size_t)(pair * 4 + split) * 4 + src;
      z[j] = __builtin_amdgcn_raw_buffer_load_b128(RSRC(ws + WS_PO + slab * 8192, 8192), w * 1024 + lane * 16, 0, 16);
      ls[j] = __hip_atomic_load(reinterpret_cast<const float*>(ws + WS_LSE + slab * 128) + r, RLX_AGENT);
    }
    SB();
    f32x4 y0 = *reinterpret_cast<const f32x4*>(smem + L_Q + 2 * w * 1024 + lane * 16);
    f32x4 y1 = *reinterpret_cast<const f32x4*>(smem + L_Q + (2 * w + 1) * 1024 + lane * 16);
    const float lo = *reinterpret_cast<const float*>(smem + L_MISC + r * 4);
    const float mx = fmaxf(fmaxf(lo, ls[0]), fmaxf(ls[1], ls[2]));
    const float w0 = __builtin_amdgcn_exp2f(lo - mx);
    float wsum = w0;
    y0 *= w0; y1 *= w0;
#pragma unroll
    for (int j = 0; j < 3; ++j) {
      const float wj = __builtin_amdgcn_exp2f(ls[j] - mx);
      wsum += wj;
      y0 += f32x4{bf_lo(z[j][0]), bf_hi(z[j][0]), bf_lo(z[j][1]), bf_hi(z[j][1])} * wj;
      y1 += f32x4{bf_lo(z[j][2]), bf_hi(z[j][2]), bf_lo(z[j][3]), bf_hi(z[j][3])} * wj;
    }
    const float iw = 1.0f / wsum;
    y0 *= iw; y1 *= iw;
    u32x4 ov = {pk_bf16(y0[0], y0[1]), pk_bf16(y0[2], y0[3]), pk_bf16(y1[0], y1[1]), pk_bf16(y1[2], y1[3])};
    __hip_bfloat16* op = out + ((size_t)(pair * REP + split) * SQ + r) * DH + 32 * (w >> 1) + 16 * h + 8 * (w & 1);
    *reinterpret_cast<u32x4*>(op) = ov;
  }
  if (split == 0 && tid == 0) __hip_atomic_store(sync, epoch, RLX_AGENT);
}

extern "C" void kernel_launch(void* const* d_in, const int* in_sizes, int n_in, void* d_out, int out_size, void* d_ws,
                              size_t ws_size, hipStream_t stream) {
  const __hip_bfloat16* q = (const __hip_bfloat16*)d_in[0];
  const int* qk = (const int*)d_in[1];
  const int* qv = (const int*)d_in[2];
  const __hip_bfloat16* ksc = (const __hip_bfloat16*)d_in[3];
  const __hip_bfloat16* kzr = (const __hip_bfloat16*)d_in[4];
  const __hip_bfloat16* vsc = (const __hip_bfloat16*)d_in[5];
  const __hip_bfloat16* vzr = (const __hip_bfloat16*)d_in[6];
  (void)hipFuncSetAttribute((const void*)kivi_attn, hipFuncAttributeMaxDynamicSharedMemorySize, L_END);
  hipLaunchKernelGGL(kivi_attn, dim3(NPAIR * NSPLIT), dim3(512), L_END, stream, q, qk, qv, ksc, kzr, vsc, vzr, (char*)d_ws,
                     (__hip_bfloat16*)d_out);
}
```

```cpp
#include <hip/hip_runtime.h>
#include <hip/hip_bf16.h>
#include <stdint.h>

typedef __attribute__((ext_vector_type(8))) __bf16 bf16x8;
typedef __attribute__((ext_vector_type(2))) __bf16 bf16x2;
typedef __attribute__((ext_vector_type(16))) float f32x16;
typedef __attribute__((ext_vector_type(4))) float f32x4;
typedef __attribute__((ext_vector_type(2))) float f32x2;
typedef __attribute__((ext_vector_type(4))) unsigned u32x4;
typedef __attribute__((ext_vector_type(2))) unsigned u32x2;
typedef unsigned long long u64;

constexpr int NB = 8, SQ = 32, SKV = 8192, HQ = 32, HKV = 8, DH = 128, GRP = 64, NGK = SKV / GRP;
constexpr int REP = HQ / HKV;
constexpr int NSPLIT = 4;
constexpr int TILE = GRP;
constexpr int NT = SKV / TILE / NSPLIT;
constexpr int NPAIR = NB * HKV;
constexpr int TILE_BYTES = TILE * DH * 4;
constexpr int CHUNK_BYTES = NT * TILE_BYTES;
constexpr int WIN = 5, AHEAD = 8, ENDT = 8;
static_assert(REP == 4 && NSPLIT == 4 && WIN <= AHEAD && AHEAD <= 12, "structure");

constexpr int KROW = 272;
constexpr int VROW = 144;
constexpr int L_K = 0;
constexpr int L_V = L_K + TILE * KROW;
constexpr int L_KC1 = L_V + DH * VROW;
constexpr int L_KC0 = L_KC1 + NT * DH * 4;
constexpr int L_VC1 = L_KC0 + NT * DH * 4;
constexpr int L_VC0 = L_VC1 + NT * TILE * 4;
constexpr int L_Q = L_VC0 + NT * TILE * 4;
constexpr int L_LSE = L_Q + 128 * KROW;
constexpr int L_END = L_LSE + 128;
static_assert(L_END <= 160 * 1024 && L_END > 80 * 1024, "LDS: exactly one workgroup per CU");

constexpr float SL2E = 0.08838834764831845f * 1.4426950408889634f;
constexpr float THR = 8.0f;
constexpr float TWO24 = 16777216.0f;

constexpr size_t WS_SYNC = 0;
constexpr size_t WS_LSE = WS_SYNC + (size_t)NPAIR * 1024;
constexpr size_t WS_PO = WS_LSE + (size_t)NPAIR * 16 * 128;

__device__ __forceinline__ float bf_lo(unsigned x) { return __builtin_bit_cast(float, x << 16); }
__device__ __forceinline__ float bf_hi(unsigned x) { return __builtin_bit_cast(float, x & 0xffff0000u); }
__device__ __forceinline__ unsigned pk_bf16(float lo, float hi) {
  f32x2 v = {lo, hi};
  return __builtin_bit_cast(unsigned, __builtin_convertvector(v, bf16x2));
}
__device__ __forceinline__ float deq(unsigned code, float c1, float c0) {
  float r;
  asm("v_fma_mix_f32 %0, %1, %2, %3 op_sel_hi:[1,0,0]" : "=v"(r) : "v"(code), "v"(c1), "v"(c0));
  return r;
}
__device__ __forceinline__ void mk_consts(unsigned s01, unsigned s23, unsigned z01, unsigned z23, f32x4& c1, f32x4& c0) {
  const float s0 = bf_lo(s01), s1 = bf_hi(s01), s2 = bf_lo(s23), s3 = bf_hi(s23);
  c1 = f32x4{s0 * TWO24, s1 * TWO24, s2 * TWO24, s3 * TWO24};
  c0 = f32x4{-bf_lo(z01) * s0, -bf_hi(z01) * s1, -bf_lo(z23) * s2, -bf_hi(z23) * s3};
}
__device__ __forceinline__ float xh_max(float x) {
  auto t = __builtin_amdgcn_permlane32_swap(__float_as_uint(x), __float_as_uint(x), false, false);
  return fmaxf(__uint_as_float(t[0]), __uint_as_float(t[1]));
}
__device__ __forceinline__ float xh_sum(float x) {
  auto t = __builtin_amdgcn_permlane32_swap(__float_as_uint(x), __float_as_uint(x), false, false);
  return __uint_as_float(t[0]) + __uint_as_float(t[1]);
}
__device__ __forceinline__ float max16(const f32x16& a) {
  float m0 = fmaxf(fmaxf(a[0], a[1]), a[2]), m1 = fmaxf(fmaxf(a[3], a[4]), a[5]), m2 = fmaxf(fmaxf(a[6], a[7]), a[8]);
  float m3 = fmaxf(fmaxf(a[9], a[10]), a[11]), m4 = fmaxf(fmaxf(a[12], a[13]), a[14]);
  return fmaxf(fmaxf(fmaxf(m0, m1), m2), fmaxf(fmaxf(m3, m4), a[15]));
}
#define RLX_AGENT __ATOMIC_RELAXED, __HIP_MEMORY_SCOPE_AGENT
#define SB() __builtin_amdgcn_sched_barrier(0)
#define BARRIER()                                          \
  do {                                                     \
    asm volatile("s_waitcnt lgkmcnt(0)" ::: "memory");     \
    __builtin_amdgcn_s_barrier();                          \
    asm volatile("" ::: "memory");                         \
  } while (0)
#define RSRC(p, n) __builtin_amdgcn_make_buffer_rsrc((void*)(p), 0, (n), 0x00020000)
constexpr int AUX_NT = 2, AUX_DEV = 16;

__global__ __launch_bounds__(512, 2) void kivi_attn(const __hip_bfloat16* __restrict__ q, const int* __restrict__ qk,
                                                    const int* __restrict__ qv, const __hip_bfloat16* __restrict__ ksc,
                                                    const __hip_bfloat16* __restrict__ kzr, const __hip_bfloat16* __restrict__ vsc,
                                                    const __hip_bfloat16* __restrict__ vzr, char* __restrict__ ws,
                                                    __hip_bfloat16* __restrict__ out) {
  extern __shared__ __attribute__((aligned(16))) char smem[];
  const int tid = threadIdx.x, lane = tid & 63, r = lane & 31, h = lane >> 5;
  const int w = __builtin_amdgcn_readfirstlane(tid >> 6);
  const bool producer = w < 4;
  const int p = w & 3;
  const int bid = blockIdx.x;
  const int pair = (bid & 7) * 8 + (bid >> 5), split = (bid >> 3) & 3;
  const int rot = bid >> 3;
#define TID(T) (((T) + rot) & (NT - 1))
  u64* sync = reinterpret_cast<u64*>(ws + WS_SYNC + (size_t)pair * 1024);

  int vzero = 0;
  asm volatile("" : "+v"(vzero));
  const u64 esrc = __hip_atomic_load(sync + vzero, RLX_AGENT);
  f32x16 o[4];
  float m = -__builtin_inff(), l = 0.f;

  if (producer) {
    __builtin_amdgcn_s_setprio(1);
    const size_t kvoff = ((size_t)pair * SKV + (size_t)split * NT * TILE) * DH;
    const auto rk = RSRC(qk + kvoff, CHUNK_BYTES);
    const auto rv = RSRC(qv + kvoff, CHUNK_BYTES);
    const int kvo = p * 8192 + lane * 16;
    const int vvo = p * 8192 + h * 2048 + r * 16;
    u32x4 rg[16];
    int ko_, vo_;
#define TOFF(T) do { const int t_ = (T); const int so_ = t_ < NT ? TID(t_) * TILE_BYTES : CHUNK_BYTES; ko_ = kvo + so_; vo_ = vvo + so_; } while (0)
#define LD(N)                                                                                                      \
  do {                                                                                                             \
    if ((N) < 4) rg[N] = __builtin_amdgcn_raw_buffer_load_b128(rk, ko_ + (N) * 1024, 0, AUX_NT);                   \
    else if ((N) < 8) rg[N] = __builtin_amdgcn_raw_buffer_load_b128(rk, ko_ + ((N) - 4) * 1024, 4096, AUX_NT);     \
    else if ((N) < 12) rg[N] = __builtin_amdgcn_raw_buffer_load_b128(rv, vo_ + ((N) - 8) * 512, 0, AUX_NT);        \
    else rg[N] = __builtin_amdgcn_raw_buffer_load_b128(rv, vo_ + ((N) - 12) * 512, 4096, AUX_NT);                  \
  } while (0)
#define THROTTLE() __builtin_amdgcn_s_waitcnt(0x0F70 | (WIN - 1))
    TOFF(0);
#pragma unroll
    for (int n = 0; n < AHEAD; ++n) { if (n >= WIN) THROTTLE(); LD(n); SB(); }
    BARRIER();
    const int kw_off = L_K + (16 * p + h) * KROW + r * 8;
    const int vw_off = L_V + r * VROW + 32 * p + 16 * h;
    for (int t = 0; t < NT; ++t) {
      const int e_ = TID(t);
      const bool thr = t < NT - ENDT;
      const f32x4 kc1 = *reinterpret_cast<const f32x4*>(smem + L_KC1 + r * 16 + e_ * 512);
      const f32x4 kc0 = *reinterpret_cast<const f32x4*>(smem + L_KC0 + r * 16 + e_ * 512);
      f32x4 vc1[2], vc0[2];
#pragma unroll
      for (int a = 0; a < 2; ++a) {
        vc1[a] = *reinterpret_cast<const f32x4*>(smem + L_VC1 + (16 * p + 8 * a + 4 * h) * 4 + e_ * 256);
        vc0[a] = *reinterpret_cast<const f32x4*>(smem + L_VC0 + (16 * p + 8 * a + 4 * h) * 4 + e_ * 256);
      }
#pragma unroll
      for (int n = 0; n < 16; ++n) {
        SB();
        asm volatile("" : "+v"(rg[n]));
        if (thr) THROTTLE();
        if (n + AHEAD == 16) TOFF(t + 1);
        LD((n + AHEAD) & 15);
        SB();
        if (n < 8) {
          u32x2 o_;
          o_[0] = pk_bf16(deq(rg[n][0], kc1[0], kc0[0]), deq(rg[n][1], kc1[1], kc0[1]));
          o_[1] = pk_bf16(deq(rg[n][2], kc1[2], kc0[2]), deq(rg[n][3], kc1[3], kc0[3]));
          *reinterpret_cast<u32x2*>(smem + kw_off + 2 * n * KROW) = o_;
          if (n == 7) BARRIER();
        } else if ((n & 3) == 3) {
          const int a = (n - 8) >> 2, b = n - 3;
#pragma unroll
          for (int c = 0; c < 4; ++c) {
            u32x2 o_;
            o_[0] = pk_bf16(deq(rg[b][c], vc1[a][0], vc0[a][0]), deq(rg[b + 1][c], vc1[a][1], vc0[a][1]));
            o_[1] = pk_bf16(deq(rg[b + 2][c], vc1[a][2], vc0[a][2]), deq(rg[b + 3][c], vc1[a][3], vc0[a][3]));
            *reinterpret_cast<u32x2*>(smem + vw_off + c * 32 * VROW + 8 * a) = o_;
          }
        }
      }
      BARRIER();
    }
    __builtin_amdgcn_s_setprio(0);
  } else {
    const int ct = p * 64 + lane;
    const auto rks = RSRC(ksc + ((size_t)pair * NGK + split * NT) * DH, NT * DH * 2);
    const auto rkz = RSRC(kzr + ((size_t)pair * NGK + split * NT) * DH, NT * DH * 2);
    const auto rvs = RSRC(vsc + (size_t)pair * SKV + (size_t)split * NT * TILE, NT * TILE * 2);
    const auto rvz = RSRC(vzr + (size_t)pair * SKV + (size_t)split * NT * TILE, NT * TILE * 2);
    u32x4 ksr[2], kzr_[2];
    u32x2 vsr[2], vzr_[2];
#pragma unroll
    for (int a = 0; a < 2; ++a) {
      ksr[a] = __builtin_amdgcn_raw_buffer_load_b128(rks, (a * 256 + ct) * 16, 0, AUX_NT);
      kzr_[a] = __builtin_amdgcn_raw_buffer_load_b128(rkz, (a * 256 + ct) * 16, 0, AUX_NT);
      vsr[a] = __builtin_amdgcn_raw_buffer_load_b64(rvs, (a * 256 + ct) * 8, 0, AUX_NT);
      vzr_[a] = __builtin_amdgcn_raw_buffer_load_b64(rvz, (a * 256 + ct) * 8, 0, AUX_NT);
    }
    u32x4 q_raw[8];
    const auto rq = RSRC(q + ((size_t)pair * REP + p) * (SQ * DH), SQ * DH * 2);
#pragma unroll
    for (int i = 0; i < 8; ++i) q_raw[i] = __builtin_amdgcn_raw_buffer_load_b128(rq, lane * 16 + i * 1024, 0, AUX_NT);
    SB();
#pragma unroll
    for (int a = 0; a < 2; ++a) {
      const int i_ = a * 256 + ct;
      f32x4 x, y;
      mk_consts(ksr[a][0], ksr[a][1], kzr_[a][0], kzr_[a][1], x, y);
      *reinterpret_cast<f32x4*>(smem + L_KC1 + i_ * 32) = x;
      *reinterpret_cast<f32x4*>(smem + L_KC0 + i_ * 32) = y;
      mk_consts(ksr[a][2], ksr[a][3], kzr_[a][2], kzr_[a][3], x, y);
      *reinterpret_cast<f32x4*>(smem + L_KC1 + i_ * 32 + 16) = x;
      *reinterpret_cast<f32x4*>(smem + L_KC0 + i_ * 32 + 16) = y;
      mk_consts(vsr[a][0], vsr[a][1], vzr_[a][0], vzr_[a][1], x, y);
      *reinterpret_cast<f32x4*>(smem + L_VC1 + i_ * 16) = x;
      *reinterpret_cast<f32x4*>(smem + L_VC0 + i_ * 16) = y;
    }
    BARRIER();
#pragma unroll
    for (int i = 0; i < 8; ++i)
      *reinterpret_cast<u32x4*>(smem + L_Q + (32 * p + 4 * i + (lane >> 4)) * KROW + (lane & 15) * 16) = q_raw[i];
    asm volatile("s_waitcnt lgkmcnt(0)" ::: "memory");
    bf16x8 qf[8];
#pragma unroll
    for (int k = 0; k < 8; ++k) qf[k] = *reinterpret_cast<const bf16x8*>(smem + L_Q + (32 * p + r) * KROW + (2 * k + h) * 16);
#pragma unroll
    for (int c = 0; c < 4; ++c) o[c] = f32x16{};
    const int kr_off = L_K + r * KROW + h * 16;
    const int vr_off = L_V + r * VROW + h * 16;
    for (int t = 0; t < NT; ++t) {
      BARRIER();
      bf16x8 fr[16];
#pragma unroll
      for (int k = 0; k < 8; ++k)
#pragma unroll
        for (int th = 0; th < 2; ++th) fr[2 * k + th] = *reinterpret_cast<const bf16x8*>(smem + kr_off + th * 32 * KROW + k * 32);
      SB();
      f32x16 s[2] = {f32x16{}, f32x16{}};
#pragma unroll
      for (int k = 0; k < 8; ++k)
#pragma unroll
        for (int th = 0; th < 2; ++th) s[th] = __builtin_amdgcn_mfma_f32_32x32x16_bf16(fr[2 * k + th], qf[k], s[th], 0, 0, 0);
      SB();
      f32x16 pp[2];
#pragma unroll
      for (int th = 0; th < 2; ++th)
#pragma unroll
        for (int i = 0; i < 16; ++i) pp[th][i] = fmaf(s[th][i], SL2E, -m);
      const float tm = fmaxf(max16(pp[0]), max16(pp[1]));
      if (__builtin_expect(__any(tm > THR), 0)) {
        const float mn = fmaxf(m, xh_max(fmaxf(max16(s[0]), max16(s[1])) * SL2E));
        const float al = __builtin_amdgcn_exp2f(m - mn);
        m = mn; l *= al;
#pragma unroll
        for (int c = 0; c < 4; ++c) o[c] *= al;
#pragma unroll
        for (int th = 0; th < 2; ++th)
#pragma unroll
          for (int i = 0; i < 16; ++i) pp[th][i] = fmaf(s[th][i], SL2E, -m);
      }
      float ls0 = 0.f, ls1 = 0.f;
      bf16x8 pf[4];
#pragma unroll
      for (int ks = 0; ks < 4; ++ks) {
        const int th = ks >> 1, b = 8 * (ks & 1);
        u32x4 pw;
#pragma unroll
        for (int i = 0; i < 4; ++i) {
          const float e0 = __builtin_amdgcn_exp2f(pp[th][b + 2 * i]), e1 = __builtin_amdgcn_exp2f(pp[th][b + 2 * i + 1]);
          ls0 += e0; ls1 += e1;
          pw[i] = pk_bf16(e0, e1);
        }
        pf[ks] = __builtin_bit_cast(bf16x8, pw);
      }
      l += ls0 + ls1;
      asm volatile("" : "+v"(pf[0]), "+v"(pf[1]), "+v"(pf[2]), "+v"(pf[3]), "+v"(l));
      SB();
      BARRIER();
#pragma unroll
      for (int ks = 0; ks < 4; ++ks)
#pragma unroll
        for (int c = 0; c < 4; ++c) fr[4 * ks + c] = *reinterpret_cast<const bf16x8*>(smem + vr_off + c * 32 * VROW + ks * 32);
      SB();
#pragma unroll
      for (int ks = 0; ks < 4; ++ks)
#pragma unroll
        for (int c = 0; c < 4; ++c) o[c] = __builtin_amdgcn_mfma_f32_32x32x16_bf16(fr[4 * ks + c], pf[ks], o[c], 0, 0, 0);
      SB();
    }
  }

  const u64 epoch = esrc * 0x9E3779B97F4A7C15ull + 0xD1B54A32D192ED03ull;
  if (!producer) {
    l = xh_sum(l);
    const float inv = 1.0f / l;
    const float lse = m + __builtin_amdgcn_logf(l);
    if (p == split) {
#pragma unroll
      for (int k = 0; k < 16; ++k)
        *reinterpret_cast<f32x4*>(smem + L_Q + k * 1024 + lane * 16) = f32x4{o[0][k] * inv, o[1][k] * inv, o[2][k] * inv, o[3][k] * inv};
      if (h == 0) *reinterpret_cast<float*>(smem + L_LSE + r * 4) = lse;
    } else {
      const size_t slab = (size_t)(pair * 4 + p) * 4 + split;
      const auto rpo = RSRC(ws + WS_PO + slab * 8192, 8192);
#pragma unroll
      for (int j = 0; j < 8; ++j) {
        u32x4 x = {pk_bf16(o[0][2 * j] * inv, o[1][2 * j] * inv), pk_bf16(o[2][2 * j] * inv, o[3][2 * j] * inv),
                   pk_bf16(o[0][2 * j + 1] * inv, o[1][2 * j + 1] * inv), pk_bf16(o[2][2 * j + 1] * inv, o[3][2 * j + 1] * inv)};
        __builtin_amdgcn_raw_buffer_store_b128(x, rpo, j * 1024 + lane * 16, 0, AUX_DEV);
      }
      if (h == 0) __hip_atomic_store(reinterpret_cast<float*>(ws + WS_LSE + slab * 128) + r, lse, RLX_AGENT);
      asm volatile("s_waitcnt vmcnt(0)" ::: "memory");
      if (lane == 0) __hip_atomic_store(sync + 16 * (1 + p) + split, epoch, RLX_AGENT);
    }
  } else if (p == 0) {
    const bool need = (lane & 3) != split;
    u64* fl = sync + 16 * (1 + split) + (lane & 3);
#pragma nounroll
    for (int spin = 0; spin < (1 << 22); ++spin) {
      const u64 v = __hip_atomic_load(fl, RLX_AGENT);
      if (__all(!need || v == epoch)) break;
      __builtin_amdgcn_s_sleep(2);
    }
  }
  BARRIER();
  {
    u32x4 z[3];
    float ls[3];
#pragma unroll
    for (int j = 0; j < 3; ++j) {
      const int src = j + (j >= split ? 1 : 0);
      const size_t slab = (size_t)(pair * 4 + split) * 4 + src;
      z[j] = __builtin_amdgcn_raw_buffer_load_b128(RSRC(ws + WS_PO + slab * 8192, 8192), w * 1024 + lane * 16, 0, AUX_DEV);
      ls[j] = __hip_atomic_load(reinterpret_cast<const float*>(ws + WS_LSE + slab * 128) + r, RLX_AGENT);
    }
    SB();
    f32x4 y0 = *reinterpret_cast<const f32x4*>(smem + L_Q + 2 * w * 1024 + lane * 16);
    f32x4 y1 = *reinterpret_cast<const f32x4*>(smem + L_Q + (2 * w + 1) * 1024 + lane * 16);
    const float lo = *reinterpret_cast<const float*>(smem + L_LSE + r * 4);
    const float mx = fmaxf(fmaxf(lo, ls[0]), fmaxf(ls[1], ls[2]));
    const float w0 = __builtin_amdgcn_exp2f(lo - mx);
    float wsum = w0;
    y0 *= w0; y1 *= w0;
#pragma unroll
    for (int j = 0; j < 3; ++j) {
      const float wj = __builtin_amdgcn_exp2f(ls[j] - mx);
      wsum += wj;
      y0 += f32x4{bf_lo(z[j][0]), bf_hi(z[j][0]), bf_lo(z[j][1]), bf_hi(z[j][1])} * wj;
      y1 += f32x4{bf_lo(z[j][2]), bf_hi(z[j][2]), bf_lo(z[j][3]), bf_hi(z[j][3])} * wj;
    }
    const float iw = 1.0f / wsum;
    y0 *= iw; y1 *= iw;
    u32x4 ov = {pk_bf16(y0[0], y0[1]), pk_bf16(y0[2], y0[3]), pk_bf16(y1[0], y1[1]), pk_bf16(y1[2], y1[3])};
    __hip_bfloat16* op = out + ((size_t)(pair * REP + split) * SQ + r) * DH + 32 * (w >> 1) + 16 * h + 8 * (w & 1);
    *reinterpret_cast<u32x4*>(op) = ov;
  }
  if (split == 0 && tid == 0) __hip_atomic_store(sync, epoch, RLX_AGENT);
}

extern "C" void kernel_launch(void* const* d_in, const int* in_sizes, int n_in, void* d_out, int out_size, void* d_ws,
                              size_t ws_size, hipStream_t stream) {
  const __hip_bfloat16* q = (const __hip_bfloat16*)d_in[0];
  const int* qk = (const int*)d_in[1];
  const int* qv = (const int*)d_in[2];
  const __hip_bfloat16* ksc = (const __hip_bfloat16*)d_in[3];
  const __hip_bfloat16* kzr = (const __hip_bfloat16*)d_in[4];
  const __hip_bfloat16* vsc = (const __hip_bfloat16*)d_in[5];
  const __hip_bfloat16* vzr = (const __hip_bfloat16*)d_in[6];
  (void)hipFuncSetAttribute((const void*)kivi_attn, hipFuncAttributeMaxDynamicSharedMemorySize, L_END);
  hipLaunchKernelGGL(kivi_attn, dim3(NPAIR * NSPLIT), dim3(512), L_END, stream, q, qk, qv, ksc, kzr, vsc, vzr, (char*)d_ws,
                     (__hip_bfloat16*)d_out);
}
```
